```python
import torch

def _softmax(x, axis=-1):
    return torch.softmax(x, dim=axis)

def _gelu(x):
    return torch.nn.functional.gelu(x, approximate='tanh')

def _silu(x):
    return torch.nn.functional.silu(x)

def _sigmoid(x):
    return torch.sigmoid(x)

def _layernorm(x, axis=-1, eps=1e-05):
    m = x.mean(dim=axis, keepdim=True)
    v = x.var(dim=axis, keepdim=True, unbiased=False)
    return (x - m) / torch.sqrt(v + eps)

def _scatter_add(tgt, idx, src, axis=0):
    return tgt.clone().index_add_(axis, idx, src)

def _segment_sum(src, seg, n):
    out = torch.zeros((n,) + src.shape[1:], dtype=src.dtype)
    return out.index_add_(0, seg, src)

def _pad(x, pw):
    flat = tuple((v for pair in reversed(pw) for v in pair))
    return torch.nn.functional.pad(x, flat)
kernel_name = 'adaLN_cond__memory_aug_recurrent_mem_tr__dense_activation_gelu__prompt'

def setup_inputs():

    _w = lambda a: (lambda t: t.to(torch.bfloat16) if t.is_floating_point() else t)(torch.as_tensor(a))

    def _A_setup():
        B, S, Dm = (1, 2048, 448)
        H, Dh = (16, 256)
        M, seg = (8, 32)
        seg = min(seg, S)
        S = S // seg * seg
        return {'x': _w((lambda *s: torch.randn(*s, dtype=torch.bfloat16) * 0.05)(B, S, Dm)), 'mem0': _w((lambda *s: torch.randn(*s, dtype=torch.bfloat16) * 0.8)(B, M, Dm)), 'Wq': _w((lambda *s: torch.randn(*s, dtype=torch.bfloat16) * 0.8)(Dm, H, Dh) * Dm ** (-0.5)), 'Wk': _w((lambda *s: torch.randn(*s, dtype=torch.bfloat16) * 0.8)(Dm, H, Dh) * Dm ** (-0.5)), 'Wv': _w((lambda *s: torch.randn(*s, dtype=torch.bfloat16) * 0.8)(Dm, H, Dh) * Dm ** (-0.5)), 'Wo': _w((lambda *s: torch.randn(*s, dtype=torch.bfloat16) * 0.8)(H, Dh, Dm) * (H * Dh) ** (-0.5)), 'seg_len': seg}
    _A_in = _A_setup()

    def _B_setup():
        B, S, D, F = (1, 2048, 448, 1792)
        return {'x': _w((lambda *s: torch.randn(*s, dtype=torch.bfloat16) * 0.05)(B, S, D)), 'w1': _w((lambda *s: torch.randn(*s, dtype=torch.bfloat16) * 0.8)(D, F) * D ** (-0.5)), 'b1': _w((lambda *s: torch.randn(*s, dtype=torch.bfloat16) * 12.8)(F) * 0.01), 'w2': _w((lambda *s: torch.randn(*s, dtype=torch.bfloat16) * 0.8)(F, D) * F ** (-0.5)), 'b2': _w((lambda *s: torch.randn(*s, dtype=torch.bfloat16) * 12.8)(D) * 0.01)}
    _B_in = _B_setup()
    return {'A_x': _A_in['x'], 'A_mem0': _A_in['mem0'], 'A_Wq': _A_in['Wq'], 'A_Wk': _A_in['Wk'], 'A_Wv': _A_in['Wv'], 'A_Wo': _A_in['Wo'], 'A_seg_len': _A_in['seg_len'], 'B_x': _B_in['x'], 'B_w1': _B_in['w1'], 'B_b1': _B_in['b1'], 'B_w2': _B_in['w2'], 'B_b2': _B_in['b2'], 'c': _w((lambda *s: torch.randn(*s, dtype=torch.bfloat16) * 0.1)(1, 448) * 8.0), 'w_mod': _w((lambda *s: torch.randn(*s, dtype=torch.bfloat16) * 0.1)(448, 6 * 448) * 448 ** (-0.5) * 8.0)}

def reference(A_x, A_mem0, A_Wq, A_Wk, A_Wv, A_Wo, A_seg_len, B_x, B_w1, B_b1, B_w2, B_b2, c, w_mod):

    def _A_ref(x, mem0, Wq, Wk, Wv, Wo, seg_len):
        B, S, Dm = x.shape
        H, Dh = (Wq.shape[1], Wq.shape[2])
        M = mem0.shape[1]
        scale = Dh ** (-0.5)
        n_seg = S // seg_len
        L = M + seg_len
        idx = torch.arange(L)
        mask = (idx[:, None] < M) | (idx[:, None] >= idx[None, :])
        mem = mem0
        outs = []
        for s in range(n_seg):
            xs = x[:, s * seg_len:(s + 1) * seg_len, :].clone()
            h = torch.concatenate([mem, xs], axis=1)
            q = torch.einsum('btd,dhe->bhte', h, Wq)
            k = torch.einsum('btd,dhe->bhte', h, Wk)
            v = torch.einsum('btd,dhe->bhte', h, Wv)
            scores = torch.einsum('bhqe,bhke->bhqk', q, k) * scale
            scores = torch.where(mask[None, None], scores, -1000000000.0)
            p = _softmax(scores, axis=-1)
            o = torch.einsum('bhqk,bhke->bhqe', p, v)
            o = torch.einsum('bhte,hed->btd', o, Wo)
            mem = o[:, :M, :].clone()
            outs.append(o[:, M:, :])
        return torch.concatenate(outs, axis=1)

    def _B_ref(x, w1, b1, w2, b2):
        h = torch.einsum('bsd,df->bsf', x, w1) + b1
        h = _gelu(h)
        return torch.einsum('bsf,fd->bsd', h, w2) + b2
    x = A_x
    mod = (_silu(c) @ w_mod).reshape(c.shape[0], 6, 1, -1)
    sa, ca, ga, sb, cb, gb = (mod[:, 0].clone(), mod[:, 1].clone(), mod[:, 2].clone(), mod[:, 3].clone(), mod[:, 4].clone(), mod[:, 5].clone())
    h = _layernorm(x, axis=-1) * (1 + ca) + sa
    x = x + ga * _A_ref(h, A_mem0, A_Wq, A_Wk, A_Wv, A_Wo, A_seg_len)
    h = _layernorm(x, axis=-1) * (1 + cb) + sb
    x = x + gb * _B_ref(h, B_w1, B_b1, B_w2, B_b2)
    return x
```

```cpp
#include <hip/hip_runtime.h>
#include <hip/hip_bf16.h>
#include <stdint.h>

typedef unsigned short u16;
typedef unsigned v4u __attribute__((ext_vector_type(4)));
typedef unsigned v2u __attribute__((ext_vector_type(2)));
typedef unsigned v8u __attribute__((ext_vector_type(8)));
typedef float f32x4 __attribute__((ext_vector_type(4)));
typedef __bf16 bf16x8 __attribute__((ext_vector_type(8)));
typedef __bf16 bf16x16 __attribute__((ext_vector_type(16)));
typedef __bf16 bf16x2 __attribute__((ext_vector_type(2)));
typedef short s16x4 __attribute__((ext_vector_type(4)));
#define DI __device__ __forceinline__
#define LAS __attribute__((address_space(3)))

constexpr int SQ = 2048, DM = 448, NH = 16, DH = 256, NM = 8, SEG = 32, NSEG = 64, FF = 1792;

constexpr size_t OFF_COMM = 0;
constexpr size_t OFF_HIST = 2u << 20;
constexpr size_t SZ_COMM = (2u << 20) + 64 * 7168;
constexpr size_t OFF_FLAGS = OFF_COMM + SZ_COMM;
constexpr size_t SZ_FLAGS = 65536;
constexpr size_t OFF_MOD = OFF_FLAGS + SZ_FLAGS;
constexpr size_t OFF_WAR = OFF_MOD + 16384;
constexpr size_t SZ_W = (size_t)NH * DM * DM * 2;
constexpr size_t OFF_WAG = OFF_WAR + SZ_W;
constexpr size_t OFF_WB = OFF_WAG + SZ_W;
constexpr size_t OFF_W1F = OFF_WB + SZ_W;
constexpr size_t SZ_WM = (size_t)DM * FF * 2;
constexpr size_t OFF_W2F = OFF_W1F + SZ_WM;
constexpr size_t OFF_HPI = OFF_W2F + SZ_WM;
constexpr size_t SZ_H = (size_t)SQ * DM * 2;
constexpr size_t OFF_HN = OFF_HPI + SZ_H;
constexpr size_t OFF_G = OFF_HN + SZ_H;
constexpr size_t SZ_GU = (size_t)SQ * NH * DM * 2;
constexpr size_t OFF_U = OFF_G + SZ_GU;
constexpr size_t OFF_SXX = OFF_U + SZ_GU;
constexpr size_t OFF_UMF = OFF_SXX + (size_t)NSEG * NH * 2 * 1024;
constexpr size_t OFF_SXM = OFF_UMF + (size_t)NSEG * 28 * 8192;
constexpr size_t OFF_X1F = OFF_SXM + (size_t)NSEG * NH * 2 * 2 * 256;
constexpr size_t OFF_FFR = OFF_X1F + (size_t)NSEG * 28 * 1024;
constexpr size_t OFF_LSE = OFF_FFR + (size_t)NSEG * 112 * 1024;
constexpr size_t WS_END = OFF_LSE + (size_t)NSEG * NH * 128;

__host__ __device__ inline unsigned chunk_off(unsigned kind, unsigned step, unsigned slot) { const unsigned per = (step & 15u) * 4u + kind; return (per << 15) + ((slot ^ (((per >> 4) & 1u) << 1)) << 13); }
DI int lane_id() { int l; asm volatile("v_mbcnt_lo_u32_b32 %0, -1, 0\n\tv_mbcnt_hi_u32_b32 %0, -1, %0" : "=v"(l)); return l; }
DI float bf2f(u16 v) { return __uint_as_float((unsigned)v << 16); }
DI float bflo(unsigned v) { return __uint_as_float(v << 16); }
DI float bfhi(unsigned v) { return __uint_as_float(v & 0xffff0000u); }
DI unsigned pk2(float a, float b) { bf16x2 r; r[0] = (__bf16)a; r[1] = (__bf16)b; return __builtin_bit_cast(unsigned, r); }
DI float rbf(float a) { return (float)(__bf16)a; }
DI f32x4 mfma(v4u a, v4u b, f32x4 c) { return __builtin_amdgcn_mfma_f32_16x16x32_bf16(__builtin_bit_cast(bf16x8, a), __builtin_bit_cast(bf16x8, b), c, 0, 0, 0); }
DI f32x4 smfma(v4u a, v8u b, f32x4 c, int idx) { return __builtin_amdgcn_smfmac_f32_16x16x64_bf16(__builtin_bit_cast(bf16x8, a), __builtin_bit_cast(bf16x16, b), c, idx, 0, 0); }
DI v2u trread(const LAS unsigned char* p) { return __builtin_bit_cast(v2u, __builtin_amdgcn_ds_read_tr16_b64_v4i16((LAS s16x4*)p)); }

struct Params {
    const u16 *x, *mem0, *Wq, *Wk, *Wv, *Wo, *w1, *b1, *w2, *b2, *c, *wmod;
    u16* out;
    unsigned char* ws;
};

constexpr int FAST0 = 60;
constexpr int K0_FORM = 224, K0_MOD = 21, K0_WGS = K0_FORM + K0_MOD;

DI unsigned char* align_ws(unsigned char* w) { return w + ((0u - (unsigned)(uintptr_t)w) & ((2u << 20) - 1u)); }
__global__ void __launch_bounds__(448) k0_prep(Params p) {
    p.ws = align_ws(p.ws);
    __shared__ __attribute__((aligned(16))) unsigned char lds[256 * 144];
    const int w = blockIdx.x, tid = threadIdx.x, lane = tid & 63, wave = __builtin_amdgcn_readfirstlane(tid >> 6);
    const int n16 = lane & 15, g = lane >> 4;
    {
        const v4u sv = {0xFFFFFFFFu, 0xFFFFFFFFu, 0xFFFFFFFFu, 0xFFFFFFFFu};
        for (size_t i = (size_t)w * 448 + tid; i < SZ_COMM / 16; i += (size_t)K0_WGS * 448) {
            v4u o = sv;
            const size_t im = i >= OFF_HIST / 16 ? i - OFF_HIST / 16 : i;
            if (im < 448) { const int pb = (int)im >> 6, l = (int)im & 63; o = *(const v4u*)(p.mem0 + (l & 7) * DM + 64 * pb + 32 * ((l >> 3) & 1) + 8 * (l >> 4));
                           o.x = min(o.x, 0xFFFFFFFEu); o.y = min(o.y, 0xFFFFFFFEu); o.z = min(o.z, 0xFFFFFFFEu); o.w = min(o.w, 0xFFFFFFFEu); }
            ((v4u*)(p.ws + OFF_COMM))[i] = o;
        }
        for (size_t i = (size_t)w * 448 + tid; i < (size_t)(NSEG - FAST0) * 28 * 64; i += (size_t)K0_WGS * 448) ((v4u*)(p.ws + OFF_X1F + (size_t)FAST0 * 28 * 1024))[i] = sv;
        for (size_t i = (size_t)w * 448 + tid; i < (size_t)(NSEG - FAST0) * 112 * 64; i += (size_t)K0_WGS * 448) ((v4u*)(p.ws + OFF_FFR + (size_t)FAST0 * 112 * 1024))[i] = sv;
        for (size_t i = (size_t)w * 448 + tid; i < (size_t)28 * 512; i += (size_t)K0_WGS * 448) ((v4u*)(p.ws + OFF_UMF + (size_t)(NSEG - 1) * 28 * 8192))[i] = sv;
        for (size_t i = (size_t)w * 448 + tid; i < (size_t)1024; i += (size_t)K0_WGS * 448) ((v4u*)(p.ws + OFF_SXM + (size_t)(NSEG - 1) * 16384))[i] = sv;
        const v4u z = {0, 0, 0, 0};
        for (size_t i = (size_t)w * 448 + tid; i < SZ_FLAGS / 16; i += (size_t)K0_WGS * 448) ((v4u*)(p.ws + OFF_FLAGS))[i] = z;
    }
    if (w < K0_FORM) {
        const int pr = (w & 7) * 4 + (w >> 3) / 7, cb = (w >> 3) % 7;
        const int mat = pr >> 4, h = pr & 15;
        v4u stg[5];
        if (mat == 0) {
#pragma unroll
            for (int k = 0; k < 5; ++k) { const int i = tid + k * 448; if (i < 2048) stg[k] = *(const v4u*)(p.Wq + ((size_t)(64 * cb + (i >> 5)) * NH + h) * DH + 8 * (i & 31)); }
        } else {
#pragma unroll
            for (int k = 0; k < 5; ++k) { const int i = tid + k * 448; if (i < 2048) stg[k] = *(const v4u*)(p.Wo + ((size_t)h * DH + (i >> 3)) * DM + 64 * cb + 8 * (i & 7)); }
        }
        const u16* W1 = (mat == 0 ? p.Wk : p.Wv) + ((size_t)(64 * wave + n16) * NH + h) * DH + 8 * g;
        v4u af[4][8];
#pragma unroll
        for (int i = 0; i < 2; ++i)
#pragma unroll
            for (int ks = 0; ks < 8; ++ks) af[i][ks] = *(const v4u*)(W1 + (size_t)(32 * i) * NH * DH + 32 * ks);
#pragma unroll
        for (int k = 0; k < 5; ++k) { const int i = tid + k * 448; if (i < 2048) *(v4u*)(lds + (mat == 0 ? (i >> 5) * 528 + (i & 31) * 16 : (i >> 3) * 144 + (i & 7) * 16)) = stg[k]; }
        __syncthreads();
#pragma unroll
        for (int i = 2; i < 4; ++i)
#pragma unroll
            for (int ks = 0; ks < 8; ++ks) af[i][ks] = *(const v4u*)(W1 + (size_t)(32 * (i - 2) + 16) * NH * DH + 32 * ks);
        const int q = n16 >> 2, pp = n16 & 3;
        const float scl = mat == 0 ? (0.0625f * 1.4426950408889634f) : 1.f;
#pragma unroll
        for (int u = 0; u < 2; ++u) {
            __builtin_amdgcn_sched_barrier(0);
            f32x4 acc[2][4];
#pragma unroll
            for (int i = 0; i < 2; ++i)
#pragma unroll
                for (int t = 0; t < 4; ++t) acc[i][t] = (f32x4){0, 0, 0, 0};
#pragma unroll
            for (int ks = 0; ks < 8; ++ks) {
                v4u bf[4];
                if (mat == 0) {
#pragma unroll
                    for (int t = 0; t < 4; ++t) bf[t] = *(const v4u*)(lds + (16 * t + n16) * 528 + (32 * ks + 8 * g) * 2);
                } else {
#pragma unroll
                    for (int t = 0; t < 4; ++t) {
                        const LAS unsigned char* a = (const LAS unsigned char*)lds + (32 * ks + 8 * g + q) * 144 + (16 * t + 4 * pp) * 2;
                        const v2u lo = trread(a), hi = trread(a + 4 * 144);
                        bf[t] = (v4u){lo.x, lo.y, hi.x, hi.y};
                    }
                }
#pragma unroll
                for (int i = 0; i < 2; ++i)
#pragma unroll
                    for (int t = 0; t < 4; ++t) acc[i][t] = mfma(af[2 * u + i][ks], bf[t], acc[i][t]);
            }
#pragma unroll
            for (int t = 0; t < 4; ++t) {
                const f32x4 x = acc[0][t] * scl, y = acc[1][t] * scl;
                const v4u o = {pk2(x[0], x[1]), pk2(y[0], y[1]), pk2(x[2], x[3]), pk2(y[2], y[3])};
                if (mat == 0) {
                    const int cl = 16 * t + n16, hi = cl >> 5, wl = cl & 31, gp = wl >> 3, r4 = wl & 7;
                    const int tR = hi + 2 * (r4 >> 2), nR = 4 * gp + (r4 & 3);
                    const size_t fragR = (((((size_t)h * 7 + cb) * 4 + tR) * 7 + wave) * 2 + u);
                    __builtin_nontemporal_store(o, (v4u*)(p.ws + OFF_WAR + fragR * 1024 + (g * 16 + nR) * 16));
                } else {
                    const size_t fragN = ((((size_t)h * 28 + 4 * cb + t) * 7 + wave) * 2 + u);
                    __builtin_nontemporal_store(o, (v4u*)(p.ws + OFF_WB + fragN * 1024 + lane * 16));
                }
            }
        }
    } else {
        const int cbk = w - K0_FORM, cg = tid & 15, kg = tid >> 4;
        v4u wv[16];
#pragma unroll
        for (int kk = 0; kk < 16; ++kk) wv[kk] = *(const v4u*)(p.wmod + (size_t)(kg * 16 + kk) * 2688 + cbk * 128 + cg * 8);
        float a[8];
#pragma unroll
        for (int j = 0; j < 8; ++j) a[j] = 0.f;
#pragma unroll
        for (int kk = 0; kk < 16; ++kk) {
            const float cv = bf2f(p.c[kg * 16 + kk]);
            const float sv = rbf(cv / (1.f + __expf(-cv)));
            a[0] += sv * bflo(wv[kk].x); a[1] += sv * bfhi(wv[kk].x); a[2] += sv * bflo(wv[kk].y); a[3] += sv * bfhi(wv[kk].y);
            a[4] += sv * bflo(wv[kk].z); a[5] += sv * bfhi(wv[kk].z); a[6] += sv * bflo(wv[kk].w); a[7] += sv * bfhi(wv[kk].w);
        }
        float* red = (float*)lds;
#pragma unroll
        for (int j = 0; j < 8; ++j) red[kg * 128 + cg * 8 + j] = a[j];
        __syncthreads();
        if (tid < 128) {
            float sv = 0.f;
            for (int k = 0; k < 28; ++k) sv += red[k * 128 + tid];
            ((float*)(p.ws + OFF_MOD))[cbk * 128 + tid] = rbf(sv);
        }
    }
}

constexpr unsigned SENT = 0xFFFFFFFFu;
constexpr int RSTR = 4096;
constexpr int L_WB = 0, L_MEM = 98304, L_RED = L_MEM + 16384, L_TM = L_RED + 4 * RSTR, L_LSE = L_TM + 64, L_ST = L_TM + 256, L_REC_END = L_ST + 1024;
constexpr int FL_SLOT = 32;
constexpr int FL_TMO = 0;
constexpr int FL_GU = 2048;
constexpr int FL_H = 2048 + 24 * 32;
constexpr int FL_REC = 64;
DI v4u ld16(__amdgpu_buffer_rsrc_t r, unsigned off) { return __builtin_amdgcn_raw_buffer_load_b128(r, off, 0, 16); }
DI v2u ld8(__amdgpu_buffer_rsrc_t r, unsigned off) { return __builtin_amdgcn_raw_buffer_load_b64(r, off, 0, 16); }
DI void st16(__amdgpu_buffer_rsrc_t r, unsigned off, v4u v) { __builtin_amdgcn_raw_buffer_store_b128(v, r, off, 0, 16); }
DI void st8(__amdgpu_buffer_rsrc_t r, unsigned off, v2u v) { __builtin_amdgcn_raw_buffer_store_b64(v, r, off, 0, 16); }
DI v4u ld16s(__amdgpu_buffer_rsrc_t r, unsigned voff, unsigned soff) { return __builtin_amdgcn_raw_buffer_load_b128(r, voff, soff, 16); }
DI v2u ld8s(__amdgpu_buffer_rsrc_t r, unsigned voff, unsigned soff) { return __builtin_amdgcn_raw_buffer_load_b64(r, voff, soff, 16); }
DI void st16s(__amdgpu_buffer_rsrc_t r, unsigned voff, unsigned soff, v4u v) { __builtin_amdgcn_raw_buffer_store_b128(v, r, voff, soff, 16); }
DI unsigned nosent(unsigned v) { return v < 0xFFFFFFFEu ? v : 0xFFFFFFFEu; }
DI bool ok4(v4u v) { return v.x != SENT && v.y != SENT && v.z != SENT && v.w != SENT; }
DI bool ok2(v2u v) { return v.x != SENT && v.y != SENT; }
DI float xmax(float v) {
    auto a = __builtin_amdgcn_permlane32_swap(__float_as_uint(v), __float_as_uint(v), false, false);
    v = fmaxf(__uint_as_float(a[0]), __uint_as_float(a[1]));
    auto b = __builtin_amdgcn_permlane16_swap(__float_as_uint(v), __float_as_uint(v), false, false);
    return fmaxf(__uint_as_float(b[0]), __uint_as_float(b[1]));
}
DI float xsum(float v) {
    auto a = __builtin_amdgcn_permlane32_swap(__float_as_uint(v), __float_as_uint(v), false, false);
    v = __uint_as_float(a[0]) + __uint_as_float(a[1]);
    auto b = __builtin_amdgcn_permlane16_swap(__float_as_uint(v), __float_as_uint(v), false, false);
    return __uint_as_float(b[0]) + __uint_as_float(b[1]);
}
DI v4u pk8(f32x4 a, f32x4 b) { return (v4u){pk2(a[0], a[1]), pk2(a[2], a[3]), pk2(b[0], b[1]), pk2(b[2], b[3])}; }
DI void delay_cyc(int n) { for (int d = 0; d < n; ++d) asm volatile("s_nop 1" ::: "memory"); }
DI bool spin_fail(unsigned& spins, unsigned* flags) {
    asm volatile("" ::: "memory");
    if (++spins > (1u << 20)) { __hip_atomic_store(flags + FL_TMO, 1u, __ATOMIC_RELAXED, __HIP_MEMORY_SCOPE_AGENT); return true; }
    return (spins & 1023u) == 0u && __builtin_amdgcn_readfirstlane(__hip_atomic_load(flags + FL_TMO, __ATOMIC_RELAXED, __HIP_MEMORY_SCOPE_AGENT)) != 0u;
}

DI void wait_vm0() { __builtin_amdgcn_s_waitcnt(0x0F70); }
DI v8u cat8(v4u a, v4u b) { return (v8u){a.x, a.y, a.z, a.w, b.x, b.y, b.z, b.w}; }
#define D_M0 3
#define D_MW 12
#define D_MS 18
constexpr int HREC = 896;
constexpr unsigned POLL_LIM = 1u << 17;
#define REPX 1
#define HISTWG 30
#define NPROBE 2
#define RFL(x) ((unsigned)__builtin_amdgcn_readfirstlane((int)(x)))
#define REC_PK 22
#define REC_PK0 14
#define REC_UXS 1
#define REC_BLOCK_STEP() \
    asm volatile( \
        "v_mov_b64 v[188:189], 0\n\t" \
        "v_mov_b64 v[190:191], 0\n\t" \
        "v_mov_b64 v[192:193], 0\n\t" \
        "v_mov_b64 v[194:195], 0\n\t" \
        "v_mov_b64 v[196:197], 0\n\t" \
        "v_mov_b64 v[198:199], 0\n\t" \
        "v_mov_b64 v[200:201], 0\n\t" \
        "v_mov_b64 v[202:203], 0\n\t" \
        "s_mov_b32 %0, 0\n\t" \
        "s_mov_b32 %1, %75\n\t" \
        "L_dl_%=:\n\t" \
        "s_sub_u32 %1, %1, 1\n\t" \
        "s_cbranch_scc0 L_dl_%=\n\t" \
        "s_barrier\n\t" \
        "L_pm_%=:\n\t" \
        "buffer_load_dwordx4 v[160:163], %54, %73, %78 offen sc1\n\t" \
        "buffer_load_dwordx4 v[164:167], %55, %73, %78 offen sc1\n\t" \
        "s_waitcnt vmcnt(0)\n\t" \
        "L_pw_%=:\n\t" \
        "ds_write_b128 %56, v[160:163]\n\t" \
        "v_max3_u32 v228, v160, v161, v162\n\t" \
        "v_max3_u32 v229, v163, v164, v165\n\t" \
        "ds_write_b128 %56, v[164:167] offset:1024\n\t" \
        "v_max3_u32 v228, v228, v166, v167\n\t" \
        "v_max_u32 v228, v228, v229\n\t" \
        "ds_write_b128 %56, v[160:163] offset:7168\n\t" \
        "v_cmp_ne_u32 vcc, -1, v228\n\t" \
        "ds_write_b128 %56, v[164:167] offset:8192\n\t" \
        "s_cmp_eq_u64 vcc, exec\n\t" \
        "s_cbranch_scc1 L_pmok_%=\n\t" \
        "s_add_u32 %0, %0, 1\n\t" \
        "s_sleep 1\n\t" \
        "s_cmp_lt_u32 %0, 131072\n\t" \
        "s_cbranch_scc1 L_pm_%=\n\t" \
        "L_pmok_%=:\n\t" \
        "v_smfmac_f32_16x16x64_bf16 v[188:191], v[160:163], %7, %52\n\t" \
        "v_smfmac_f32_16x16x64_bf16 v[192:195], v[160:163], %14, %52\n\t" \
        "v_smfmac_f32_16x16x64_bf16 v[196:199], v[160:163], %21, %52\n\t" \
        "v_smfmac_f32_16x16x64_bf16 v[200:203], v[160:163], %28, %52\n\t" \
        "s_waitcnt lgkmcnt(0)\n\t" \
        "s_barrier\n\t" \
        "ds_read_b128 v[168:171], %56 offset:2048\n\t" \
        "ds_read_b128 v[172:175], %56 offset:3072\n\t" \
        "ds_read_b128 v[176:179], %56 offset:4096\n\t" \
        "ds_read_b128 v[180:183], %56 offset:5120\n\t" \
        "ds_read_b128 v[184:187], %56 offset:6144\n\t" \
        "ds_read_b128 v[220:223], %57\n\t" \
        "v_smfmac_f32_16x16x64_bf16 v[188:191], v[164:167], %8, %52\n\t" \
        "s_add_u32 %1, %74, 1\n\t" \
        "v_smfmac_f32_16x16x64_bf16 v[192:195], v[164:167], %15, %52\n\t" \
        "v_mov_b32 v159, %1\n\t" \
        "v_smfmac_f32_16x16x64_bf16 v[196:199], v[164:167], %22, %52\n\t" \
        "v_smfmac_f32_16x16x64_bf16 v[200:203], v[164:167], %29, %52\n\t" \
        "s_waitcnt lgkmcnt(5)\n\t" \
        "v_smfmac_f32_16x16x64_bf16 v[188:191], v[168:171], %9, %52\n\t" \
        "v_smfmac_f32_16x16x64_bf16 v[192:195], v[168:171], %16, %52\n\t" \
        "v_smfmac_f32_16x16x64_bf16 v[196:199], v[168:171], %23, %52\n\t" \
        "v_smfmac_f32_16x16x64_bf16 v[200:203], v[168:171], %30, %52\n\t" \
        "s_waitcnt lgkmcnt(4)\n\t" \
        "v_smfmac_f32_16x16x64_bf16 v[188:191], v[172:175], %10, %52\n\t" \
        "v_smfmac_f32_16x16x64_bf16 v[192:195], v[172:175], %17, %52\n\t" \
        "v_smfmac_f32_16x16x64_bf16 v[196:199], v[172:175], %24, %52\n\t" \
        "v_smfmac_f32_16x16x64_bf16 v[200:203], v[172:175], %31, %52\n\t" \
        "s_waitcnt lgkmcnt(3)\n\t" \
        "v_smfmac_f32_16x16x64_bf16 v[188:191], v[176:179], %11, %52\n\t" \
        "v_smfmac_f32_16x16x64_bf16 v[192:195], v[176:179], %18, %52\n\t" \
        "s_waitcnt lgkmcnt(2)\n\t" \
        "v_smfmac_f32_16x16x64_bf16 v[188:191], v[180:183], %12, %52\n\t" \
        "v_smfmac_f32_16x16x64_bf16 v[192:195], v[180:183], %19, %52\n\t" \
        "s_waitcnt lgkmcnt(0)\n\t" \
        "v_smfmac_f32_16x16x64_bf16 v[188:191], v[184:187], %13, %52\n\t" \
        "v_smfmac_f32_16x16x64_bf16 v[192:195], v[184:187], %20, %52\n\t" \
        "v_smfmac_f32_16x16x64_bf16 v[196:199], v[176:179], %25, %52\n\t" \
        "v_smfmac_f32_16x16x64_bf16 v[200:203], v[176:179], %32, %52\n\t" \
        "v_smfmac_f32_16x16x64_bf16 v[196:199], v[180:183], %26, %52\n\t" \
        "v_smfmac_f32_16x16x64_bf16 v[200:203], v[180:183], %33, %52\n\t" \
        "v_smfmac_f32_16x16x64_bf16 v[196:199], v[184:187], %27, %52\n\t" \
        "v_smfmac_f32_16x16x64_bf16 v[200:203], v[184:187], %34, %52\n\t" \
        "s_nop 0\n\t" \
        "v_cvt_pk_bf16_f32 v228, v188, v189\n\t" \
        "v_cvt_pk_bf16_f32 v229, v190, v191\n\t" \
        "v_cvt_pk_bf16_f32 v230, v192, v193\n\t" \
        "v_cvt_pk_bf16_f32 v231, v194, v195\n\t" \
        "s_nop 1\n\t" \
        "v_mfma_f32_16x16x32_bf16 v[236:239], v[228:231], %51, 0\n\t" \
        "v_cvt_pk_bf16_f32 v232, v196, v197\n\t" \
        "v_cvt_pk_bf16_f32 v233, v198, v199\n\t" \
        "v_cvt_pk_bf16_f32 v234, v200, v201\n\t" \
        "v_cvt_pk_bf16_f32 v235, v202, v203\n\t" \
        "s_nop 1\n\t" \
        "v_mfma_f32_16x16x32_bf16 v[240:243], v[232:235], %51, 0\n\t" \
        "s_nop 0\n\t" \
        "v_cvt_pk_bf16_f32 v252, v236, v237\n\t" \
        "v_cvt_pk_bf16_f32 v253, v238, v239\n\t" \
        "s_nop 4\n\t" \
        "v_cvt_pk_bf16_f32 v254, v240, v241\n\t" \
        "v_cvt_pk_bf16_f32 v255, v242, v243\n\t" \
        "s_nop 1\n\t" \
        "v_mfma_f32_16x16x32_bf16 v[248:251], v[252:255], v[220:223], 0\n\t" \
        "v_mov_b64 v[236:237], 0\n\t" \
        "v_mov_b64 v[238:239], 0\n\t" \
        "v_mov_b64 v[240:241], 0\n\t" \
        "v_mov_b64 v[242:243], 0\n\t" \
        "v_mov_b64 v[244:245], 0\n\t" \
        "v_mov_b64 v[246:247], 0\n\t" \
        "s_nop 1\n\t" \
        "ds_write_b128 %58, v[248:251]\n\t" \
        "ds_write_b32 %61, v159\n\t" \
        "s_cmp_lg_u32 %76, 0\n\t" \
        "s_cbranch_scc1 L_np_%=\n\t" \
        "s_mov_b64 exec, 0xffff\n\t" \
        "L_rsa_%=:\n\t" \
        "ds_read_b128 v[228:231], %62 offset:0\n\t" \
        "ds_read_b128 v[188:191], %59 offset:0\n\t" \
        "ds_read_b128 v[192:195], %59 offset:640\n\t" \
        "ds_read_b128 v[196:199], %59 offset:4096\n\t" \
        "ds_read_b128 v[200:203], %59 offset:4736\n\t" \
        "ds_read_b128 v[204:207], %59 offset:8192\n\t" \
        "ds_read_b128 v[208:211], %59 offset:8832\n\t" \
        "ds_read_b128 v[212:215], %59 offset:12288\n\t" \
        "ds_read_b128 v[216:219], %59 offset:12928\n\t" \
        "s_waitcnt lgkmcnt(8)\n\t" \
        "v_min3_u32 v228, v228, v229, v230\n\t" \
        "v_min_u32 v228, v228, v231\n\t" \
        "v_cmp_lt_u32 vcc, v228, v159\n\t" \
        "s_waitcnt lgkmcnt(6)\n\t" \
        "v_pk_add_f32 v[188:189], v[188:189], v[192:193]\n\t" \
        "v_pk_add_f32 v[190:191], v[190:191], v[194:195]\n\t" \
        "s_cbranch_vccz L_rka_%=\n\t" \
        "s_waitcnt lgkmcnt(0)\n\t" \
        "s_add_u32 %0, %0, 1\n\t" \
        "s_cmp_lt_u32 %0, 131072\n\t" \
        "s_cbranch_scc1 L_rsa_%=\n\t" \
        "L_rka_%=:\n\t" \
        "s_waitcnt lgkmcnt(4)\n\t" \
        "v_pk_add_f32 v[196:197], v[196:197], v[200:201]\n\t" \
        "v_pk_add_f32 v[198:199], v[198:199], v[202:203]\n\t" \
        "v_pk_add_f32 v[188:189], v[188:189], v[196:197]\n\t" \
        "v_pk_add_f32 v[190:191], v[190:191], v[198:199]\n\t" \
        "s_waitcnt lgkmcnt(2)\n\t" \
        "v_pk_add_f32 v[204:205], v[204:205], v[208:209]\n\t" \
        "v_pk_add_f32 v[206:207], v[206:207], v[210:211]\n\t" \
        "v_pk_add_f32 v[188:189], v[188:189], v[204:205]\n\t" \
        "v_pk_add_f32 v[190:191], v[190:191], v[206:207]\n\t" \
        "s_waitcnt lgkmcnt(0)\n\t" \
        "v_pk_add_f32 v[212:213], v[212:213], v[216:217]\n\t" \
        "v_pk_add_f32 v[214:215], v[214:215], v[218:219]\n\t" \
        "v_pk_add_f32 v[188:189], v[188:189], v[212:213]\n\t" \
        "v_pk_add_f32 v[190:191], v[190:191], v[214:215]\n\t" \
        "v_cvt_pk_bf16_f32 v220, v188, v189\n\t" \
        "v_cvt_pk_bf16_f32 v221, v190, v191\n\t" \
        "v_min_u32 v220, -2, v220\n\t" \
        "v_min_u32 v221, -2, v221\n\t" \
        "buffer_store_dwordx2 v[220:221], %64, %73, %80 offen sc1\n\t" \
        "s_mov_b64 exec, -1\n\t" \
        "s_branch L_pd_%=\n\t" \
        "L_np_%=:\n\t" \
        "s_cmp_lg_u32 %76, 2\n\t" \
        "s_cbranch_scc1 L_nc_%=\n\t" \
        "s_xor_b32 %1, %80, 0x100000\n\t" \
        "buffer_store_dwordx4 %72, %70, %73, %1 offen sc1\n\t" \
        "s_xor_b32 %1, %79, 0x100000\n\t" \
        "buffer_store_dwordx4 %72, %71, %73, %1 offen sc1\n\t" \
        "L_nc_%=:\n\t" \
        "s_waitcnt lgkmcnt(0)\n\t" \
        "L_pd_%=:\n\t" \
        "ds_read_b128 v[188:191], %63 offset:0\n\t" \
        "ds_read_b128 v[192:195], %63 offset:1024\n\t" \
        "ds_read_b128 v[196:199], %63 offset:2048\n\t" \
        "ds_read_b128 v[200:203], %63 offset:3072\n\t" \
        "ds_read_b128 v[204:207], %63 offset:4096\n\t" \
        "ds_read_b128 v[208:211], %63 offset:5120\n\t" \
        "ds_read_b128 v[212:215], %63 offset:6144\n\t" \
        "ds_read_b128 v[216:219], %63 offset:7168\n\t" \
        "v_mov_b64 v[248:249], 0\n\t" \
        "v_mov_b64 v[250:251], 0\n\t" \
        "s_nop 0\n\t" \
        "v_smfmac_f32_16x16x64_bf16 v[236:239], v[160:163], %35, %52\n\t" \
        "v_smfmac_f32_16x16x64_bf16 v[240:243], v[160:163], %42, %52\n\t" \
        "v_smfmac_f32_16x16x64_bf16 v[236:239], v[164:167], %36, %52\n\t" \
        "v_smfmac_f32_16x16x64_bf16 v[240:243], v[164:167], %43, %52\n\t" \
        "v_smfmac_f32_16x16x64_bf16 v[244:247], v[160:163], %49, %52\n\t" \
        "s_waitcnt lgkmcnt(6)\n\t" \
        "v_smfmac_f32_16x16x64_bf16 v[248:251], v[160:163], v[188:195], %52\n\t" \
        "ds_read_b128 v[188:191], %63 offset:8192\n\t" \
        "ds_read_b128 v[192:195], %63 offset:9216\n\t" \
        "v_smfmac_f32_16x16x64_bf16 v[244:247], v[164:167], %50, %52\n\t" \
        "s_waitcnt lgkmcnt(6)\n\t" \
        "v_smfmac_f32_16x16x64_bf16 v[248:251], v[164:167], v[196:203], %52\n\t" \
        "ds_read_b128 v[196:199], %63 offset:10240\n\t" \
        "ds_read_b128 v[200:203], %63 offset:11264\n\t" \
        "v_smfmac_f32_16x16x64_bf16 v[236:239], v[168:171], %37, %52\n\t" \
        "s_waitcnt lgkmcnt(6)\n\t" \
        "v_smfmac_f32_16x16x64_bf16 v[244:247], v[168:171], v[204:211], %52\n\t" \
        "ds_read_b128 v[204:207], %63 offset:12288\n\t" \
        "ds_read_b128 v[208:211], %63 offset:13312\n\t" \
        "v_smfmac_f32_16x16x64_bf16 v[240:243], v[168:171], %44, %52\n\t" \
        "s_waitcnt lgkmcnt(6)\n\t" \
        "v_smfmac_f32_16x16x64_bf16 v[248:251], v[168:171], v[212:219], %52\n\t" \
        "ds_read_b128 v[212:215], %63 offset:14336\n\t" \
        "ds_read_b128 v[216:219], %63 offset:15360\n\t" \
        "v_smfmac_f32_16x16x64_bf16 v[236:239], v[172:175], %38, %52\n\t" \
        "s_waitcnt lgkmcnt(6)\n\t" \
        "v_smfmac_f32_16x16x64_bf16 v[244:247], v[172:175], v[188:195], %52\n\t" \
        "ds_read_b128 v[188:191], %63 offset:16384\n\t" \
        "ds_read_b128 v[192:195], %63 offset:17408\n\t" \
        "s_cmp_lg_u32 %77, 14\n\t" \
        "s_cbranch_scc1 L_sp14_%=\n\t" \
        "s_barrier\n\t" \
        "buffer_load_dwordx4 v[220:223], %65, %73, %81 offen sc1\n\t" \
        "buffer_load_dwordx4 v[224:227], %65, %73, %81 offen offset:1792 sc1\n\t" \
        "buffer_load_dwordx2 v[232:233], %66, %73, %81 offen sc1\n\t" \
        "buffer_load_dwordx2 v[228:229], %66, %73, %81 offen offset:128 sc1\n\t" \
        "buffer_load_dword v230, %67, %73, %81 offen sc1\n\t" \
        "L_sp14_%=:\n\t" \
        "v_smfmac_f32_16x16x64_bf16 v[240:243], v[172:175], %45, %52\n\t" \
        "s_waitcnt lgkmcnt(6)\n\t" \
        "v_smfmac_f32_16x16x64_bf16 v[248:251], v[172:175], v[196:203], %52\n\t" \
        "ds_read_b128 v[196:199], %63 offset:18432\n\t" \
        "ds_read_b128 v[200:203], %63 offset:19456\n\t" \
        "v_smfmac_f32_16x16x64_bf16 v[236:239], v[176:179], %39, %52\n\t" \
        "s_waitcnt lgkmcnt(6)\n\t" \
        "v_smfmac_f32_16x16x64_bf16 v[244:247], v[176:179], v[204:211], %52\n\t" \
        "ds_read_b128 v[204:207], %63 offset:20480\n\t" \
        "ds_read_b128 v[208:211], %63 offset:21504\n\t" \
        "v_smfmac_f32_16x16x64_bf16 v[240:243], v[176:179], %46, %52\n\t" \
        "s_waitcnt lgkmcnt(6)\n\t" \
        "v_smfmac_f32_16x16x64_bf16 v[248:251], v[176:179], v[212:219], %52\n\t" \
        "ds_read_b128 v[212:215], %63 offset:22528\n\t" \
        "ds_read_b128 v[216:219], %63 offset:23552\n\t" \
        "v_smfmac_f32_16x16x64_bf16 v[236:239], v[180:183], %40, %52\n\t" \
        "s_waitcnt lgkmcnt(6)\n\t" \
        "v_smfmac_f32_16x16x64_bf16 v[244:247], v[180:183], v[188:195], %52\n\t" \
        "s_cmp_lg_u32 %77, 22\n\t" \
        "s_cbranch_scc1 L_sp22_%=\n\t" \
        "s_barrier\n\t" \
        "buffer_load_dwordx4 v[220:223], %65, %73, %81 offen sc1\n\t" \
        "buffer_load_dwordx4 v[224:227], %65, %73, %81 offen offset:1792 sc1\n\t" \
        "buffer_load_dwordx2 v[232:233], %66, %73, %81 offen sc1\n\t" \
        "buffer_load_dwordx2 v[228:229], %66, %73, %81 offen offset:128 sc1\n\t" \
        "buffer_load_dword v230, %67, %73, %81 offen sc1\n\t" \
        "L_sp22_%=:\n\t" \
        "v_smfmac_f32_16x16x64_bf16 v[240:243], v[180:183], %47, %52\n\t" \
        "s_waitcnt lgkmcnt(4)\n\t" \
        "v_smfmac_f32_16x16x64_bf16 v[248:251], v[180:183], v[196:203], %52\n\t" \
        "v_smfmac_f32_16x16x64_bf16 v[236:239], v[184:187], %41, %52\n\t" \
        "s_waitcnt lgkmcnt(2)\n\t" \
        "v_smfmac_f32_16x16x64_bf16 v[244:247], v[184:187], v[204:211], %52\n\t" \
        "v_smfmac_f32_16x16x64_bf16 v[240:243], v[184:187], %48, %52\n\t" \
        "s_waitcnt lgkmcnt(0)\n\t" \
        "v_smfmac_f32_16x16x64_bf16 v[248:251], v[184:187], v[212:219], %52\n\t" \
        "s_nop 7\n\t" \
        "v_cvt_pk_bf16_f32 v188, v236, v237\n\t" \
        "v_cvt_pk_bf16_f32 v189, v238, v239\n\t" \
        "v_cvt_pk_bf16_f32 v192, v240, v241\n\t" \
        "v_cvt_pk_bf16_f32 v193, v242, v243\n\t" \
        "v_cvt_pk_bf16_f32 v190, v244, v245\n\t" \
        "v_cvt_pk_bf16_f32 v191, v246, v247\n\t" \
        "v_cvt_pk_bf16_f32 v194, v248, v249\n\t" \
        "v_cvt_pk_bf16_f32 v195, v250, v251\n\t" \
        "s_cmp_eq_u32 %83, 0\n\t" \
        "s_cbranch_scc1 L_nh_%=\n\t" \
        "buffer_store_dwordx4 v[160:163], %54, %73, %83 offen sc1\n\t" \
        "buffer_store_dwordx4 v[164:167], %55, %73, %83 offen sc1\n\t" \
        "s_waitcnt vmcnt(2)\n\t" \
        "s_branch L_chk_%=\n\t" \
        "L_nh_%=:\n\t" \
        "s_waitcnt vmcnt(0)\n\t" \
        "L_chk_%=:\n\t" \
        "v_mfma_f32_16x16x32_bf16 v[196:199], %2, v[220:223], 0\n\t" \
        "v_max3_u32 v212, v220, v221, v222\n\t" \
        "v_max3_u32 v213, v223, v224, v225\n\t" \
        "v_max3_u32 v212, v212, v226, v227\n\t" \
        "v_mfma_f32_16x16x32_bf16 v[200:203], %3, v[220:223], 0\n\t" \
        "v_max3_u32 v213, v213, v232, v233\n\t" \
        "v_max3_u32 v212, v212, v228, v229\n\t" \
        "v_max3_u32 v212, v212, v213, v230\n\t" \
        "v_mfma_f32_16x16x32_bf16 v[204:207], %4, v[224:227], 0\n\t" \
        "v_cmp_ne_u32 vcc, -1, v212\n\t" \
        "v_lshlrev_b32 v236, 16, v232\n\t" \
        "s_cmp_eq_u64 vcc, exec\n\t" \
        "s_cbranch_scc1 L_ok_%=\n\t" \
        "s_add_u32 %0, %0, 1\n\t" \
        "s_cmp_lt_u32 %0, 131072\n\t" \
        "s_cbranch_scc0 L_ok_%=\n\t" \
        "s_sleep 1\n\t" \
        "buffer_load_dwordx4 v[220:223], %65, %73, %81 offen sc1\n\t" \
        "buffer_load_dwordx4 v[224:227], %65, %73, %81 offen offset:1792 sc1\n\t" \
        "buffer_load_dwordx2 v[232:233], %66, %73, %81 offen sc1\n\t" \
        "buffer_load_dwordx2 v[228:229], %66, %73, %81 offen offset:128 sc1\n\t" \
        "buffer_load_dword v230, %67, %73, %81 offen sc1\n\t" \
        "s_waitcnt vmcnt(0)\n\t" \
        "s_branch L_chk_%=\n\t" \
        "L_ok_%=:\n\t" \
        "v_mfma_f32_16x16x32_bf16 v[208:211], %5, v[224:227], 0\n\t" \
        "v_and_b32 v237, 0xffff0000, v232\n\t" \
        "v_lshlrev_b32 v238, 16, v233\n\t" \
        "v_and_b32 v239, 0xffff0000, v233\n\t" \
        "v_lshlrev_b32 v240, 16, v228\n\t" \
        "v_and_b32 v241, 0xffff0000, v228\n\t" \
        "v_lshlrev_b32 v242, 16, v229\n\t" \
        "v_and_b32 v243, 0xffff0000, v229\n\t" \
        "v_pk_add_f32 v[236:237], v[236:237], v[240:241]\n\t" \
        "v_pk_add_f32 v[238:239], v[238:239], v[242:243]\n\t" \
        "v_max3_f32 v244, v236, v237, v230\n\t" \
        "v_max3_f32 v244, v244, v238, v239\n\t" \
        "v_mov_b32 v245, v244\n\t" \
        "s_nop 1\n\t" \
        "v_permlane16_swap_b32 v244, v245\n\t" \
        "v_max_f32 v244, v244, v245\n\t" \
        "v_pk_add_f32 v[236:237], v[236:237], v[244:245] op_sel_hi:[1,0] neg_lo:[0,1] neg_hi:[0,1]\n\t" \
        "v_pk_add_f32 v[238:239], v[238:239], v[244:245] op_sel_hi:[1,0] neg_lo:[0,1] neg_hi:[0,1]\n\t" \
        "v_sub_f32 v246, v230, v244\n\t" \
        "v_exp_f32 v236, v236\n\t" \
        "v_exp_f32 v237, v237\n\t" \
        "v_exp_f32 v238, v238\n\t" \
        "v_exp_f32 v239, v239\n\t" \
        "v_exp_f32 v246, v246\n\t" \
        "v_pk_add_f32 v[248:249], v[236:237], v[238:239]\n\t" \
        "v_add_f32 v248, v248, v249\n\t" \
        "v_mov_b32 v245, v248\n\t" \
        "buffer_load_dwordx4 %2, %53, %73, %82 offen sc1\n\t" \
        "s_add_u32 %1, %82, 57344\n\t" \
        "v_permlane16_swap_b32 v248, v245\n\t" \
        "v_add_f32 v248, v248, v245\n\t" \
        "v_add_f32 v248, v248, v246\n\t" \
        "v_rcp_f32 v250, v248\n\t" \
        "buffer_load_dwordx4 %3, %69, %73, %82 offen sc1\n\t" \
        "v_mul_f32 v240, v246, v250\n\t" \
        "v_mul_f32 v242, v246, v250\n\t" \
        "v_pk_mul_f32 v[236:237], v[236:237], v[250:251] op_sel_hi:[1,0]\n\t" \
        "v_pk_mul_f32 v[238:239], v[238:239], v[250:251] op_sel_hi:[1,0]\n\t" \
        "v_permlane32_swap_b32 v240, v242\n\t" \
        "v_cvt_pk_bf16_f32 v220, v236, v237\n\t" \
        "v_cvt_pk_bf16_f32 v222, v236, v237\n\t" \
        "v_cvt_pk_bf16_f32 v221, v238, v239\n\t" \
        "v_cvt_pk_bf16_f32 v223, v238, v239\n\t" \
        "v_pk_mul_f32 v[196:197], v[196:197], v[240:241] op_sel_hi:[1,0]\n\t" \
        "v_pk_mul_f32 v[198:199], v[198:199], v[240:241] op_sel_hi:[1,0]\n\t" \
        "v_permlane32_swap_b32 v220, v222\n\t" \
        "v_permlane32_swap_b32 v221, v223\n\t" \
        "v_pk_fma_f32 v[196:197], v[204:205], v[242:243], v[196:197] op_sel_hi:[1,0,1]\n\t" \
        "v_pk_fma_f32 v[198:199], v[206:207], v[242:243], v[198:199] op_sel_hi:[1,0,1]\n\t" \
        "v_pk_mul_f32 v[200:201], v[200:201], v[240:241] op_sel_hi:[1,0]\n\t" \
        "v_pk_mul_f32 v[202:203], v[202:203], v[240:241] op_sel_hi:[1,0]\n\t" \
        "v_mfma_f32_16x16x32_bf16 v[196:199], v[188:191], v[220:223], v[196:199]\n\t" \
        "v_pk_fma_f32 v[200:201], v[208:209], v[242:243], v[200:201] op_sel_hi:[1,0,1]\n\t" \
        "v_pk_fma_f32 v[202:203], v[210:211], v[242:243], v[202:203] op_sel_hi:[1,0,1]\n\t" \
        "buffer_load_dwordx4 %4, %53, %73, %1 offen sc1\n\t" \
        "buffer_load_dwordx4 %5, %69, %73, %1 offen sc1\n\t" \
        "v_mfma_f32_16x16x32_bf16 v[200:203], v[192:195], v[220:223], v[200:203]\n\t" \
        "s_nop 1\n\t" \
        "s_mov_b32 exec_lo, 0x00ff00ff\n\t" \
        "s_mov_b32 exec_hi, 0x00ff00ff\n\t" \
        "ds_write_b128 %58, v[196:199] offset:1024\n\t" \
        "s_not_b64 exec, exec\n\t" \
        "s_nop 1\n\t" \
        "ds_write_b128 %58, v[200:203] offset:1024\n\t" \
        "s_mov_b64 exec, -1\n\t" \
        "ds_write_b32 %61, v159 offset:16\n\t" \
        "s_cmp_lg_u32 %76, 0\n\t" \
        "s_cbranch_scc1 L_end_%=\n\t" \
        "s_mov_b32 exec_lo, 0x00ff00ff\n\t" \
        "s_mov_b32 exec_hi, 0x00ff00ff\n\t" \
        "L_rsb_%=:\n\t" \
        "ds_read_b128 v[228:231], %62 offset:16\n\t" \
        "ds_read_b128 v[188:191], %60 offset:0\n\t" \
        "ds_read_b128 v[192:195], %60 offset:128\n\t" \
        "ds_read_b128 v[196:199], %60 offset:4096\n\t" \
        "ds_read_b128 v[200:203], %60 offset:4224\n\t" \
        "ds_read_b128 v[204:207], %60 offset:8192\n\t" \
        "ds_read_b128 v[208:211], %60 offset:8320\n\t" \
        "ds_read_b128 v[212:215], %60 offset:12288\n\t" \
        "ds_read_b128 v[216:219], %60 offset:12416\n\t" \
        "s_waitcnt lgkmcnt(8)\n\t" \
        "v_min3_u32 v228, v228, v229, v230\n\t" \
        "v_min_u32 v228, v228, v231\n\t" \
        "v_cmp_lt_u32 vcc, v228, v159\n\t" \
        "s_waitcnt lgkmcnt(6)\n\t" \
        "v_pk_add_f32 v[188:189], v[188:189], v[192:193]\n\t" \
        "v_pk_add_f32 v[190:191], v[190:191], v[194:195]\n\t" \
        "s_cbranch_vccz L_rkb_%=\n\t" \
        "s_waitcnt lgkmcnt(0)\n\t" \
        "s_add_u32 %0, %0, 1\n\t" \
        "s_cmp_lt_u32 %0, 131072\n\t" \
        "s_cbranch_scc1 L_rsb_%=\n\t" \
        "L_rkb_%=:\n\t" \
        "s_waitcnt lgkmcnt(4)\n\t" \
        "v_pk_add_f32 v[196:197], v[196:197], v[200:201]\n\t" \
        "v_pk_add_f32 v[198:199], v[198:199], v[202:203]\n\t" \
        "v_pk_add_f32 v[188:189], v[188:189], v[196:197]\n\t" \
        "v_pk_add_f32 v[190:191], v[190:191], v[198:199]\n\t" \
        "s_waitcnt lgkmcnt(2)\n\t" \
        "v_pk_add_f32 v[204:205], v[204:205], v[208:209]\n\t" \
        "v_pk_add_f32 v[206:207], v[206:207], v[210:211]\n\t" \
        "v_pk_add_f32 v[188:189], v[188:189], v[204:205]\n\t" \
        "v_pk_add_f32 v[190:191], v[190:191], v[206:207]\n\t" \
        "s_waitcnt lgkmcnt(0)\n\t" \
        "v_pk_add_f32 v[212:213], v[212:213], v[216:217]\n\t" \
        "v_pk_add_f32 v[214:215], v[214:215], v[218:219]\n\t" \
        "v_pk_add_f32 v[188:189], v[188:189], v[212:213]\n\t" \
        "v_pk_add_f32 v[190:191], v[190:191], v[214:215]\n\t" \
        "v_cvt_pk_bf16_f32 v220, v188, v189\n\t" \
        "v_cvt_pk_bf16_f32 v221, v190, v191\n\t" \
        "v_min_u32 v220, -2, v220\n\t" \
        "v_min_u32 v221, -2, v221\n\t" \
        "buffer_store_dwordx2 v[220:221], %68, %73, %79 offen sc1\n\t" \
        "s_mov_b64 exec, -1\n\t" \
        "L_end_%=:\n\t" \
        "s_nop 1\n\t" \
        : "=&s"(cnt), "=&s"(st), "+v"(ux[0]), "+v"(ux[1]), "+v"(ux[2]), "+v"(ux[3]), "+v"(dmy) \
        : "a"(wa[0][0]), "a"(wa[0][1]), "a"(wa[0][2]), "a"(wa[0][3]), "a"(wa[0][4]), "a"(wa[0][5]), "a"(wa[0][6]), "a"(wa[1][0]), "a"(wa[1][1]), "a"(wa[1][2]), "a"(wa[1][3]), "a"(wa[1][4]), "a"(wa[1][5]), "a"(wa[1][6]), "a"(wa[2][0]), "a"(wa[2][1]), "a"(wa[2][2]), "a"(wa[2][3]), "a"(wa[2][4]), "a"(wa[2][5]), "a"(wa[2][6]), "a"(wa[3][0]), "a"(wa[3][1]), "a"(wa[3][2]), "a"(wa[3][3]), "a"(wa[3][4]), "a"(wa[3][5]), "a"(wa[3][6]), "a"(wb[0][0]), "a"(wb[0][1]), "a"(wb[0][2]), "a"(wb[0][3]), "v"(wb[0][4]), "v"(wb[0][5]), "v"(wb[0][6]), "v"(wb[1][0]), "v"(wb[1][1]), "v"(wb[1][2]), "v"(wb[1][3]), "v"(wb[1][4]), "v"(wb[1][5]), "v"(wb[1][6]), "v"(wb[2][0]), "v"(wb[2][1]), "v"(bsel), "v"(sidx), "v"(gL16), "v"(gM0), "v"(gM1), "v"(aMr), "v"(aMb), "v"(aW), "v"(aR1), "v"(aRB), "v"(aFl), "v"(aFr), "v"(aWB), "v"(gPub), "v"(gE), "v"(gS0), "v"(gS1), "v"(gPM), "v"(gU1), "v"(gC1), "v"(gC2), "v"(ones), "s"(rs), "s"(s), "s"(dly), "s"(wave), "s"(pk), "s"(sM), "s"(sMn), "s"(sW), "s"(sR), "s"(sUX), "s"(sHc) \
        : "v159", "v160", "v161", "v162", "v163", "v164", "v165", "v166", "v167", "v168", "v169", "v170", "v171", "v172", "v173", "v174", "v175", "v176", "v177", "v178", "v179", "v180", "v181", "v182", "v183", "v184", "v185", "v186", "v187", "v188", "v189", "v190", "v191", "v192", "v193", "v194", "v195", "v196", "v197", "v198", "v199", "v200", "v201", "v202", "v203", "v204", "v205", "v206", "v207", "v208", "v209", "v210", "v211", "v212", "v213", "v214", "v215", "v216", "v217", "v218", "v219", "v220", "v221", "v222", "v223", "v224", "v225", "v226", "v227", "v228", "v229", "v230", "v231", "v232", "v233", "v234", "v235", "v236", "v237", "v238", "v239", "v240", "v241", "v242", "v243", "v244", "v245", "v246", "v247", "v248", "v249", "v250", "v251", "v252", "v253", "v254", "v255", "vcc", "scc", "memory");

#define REC_SMX_STEP() \
    asm volatile( \
        "v_mov_b64 v[188:189], 0\n\t" \
        "v_mov_b64 v[190:191], 0\n\t" \
        "v_mov_b64 v[192:193], 0\n\t" \
        "v_mov_b64 v[194:195], 0\n\t" \
        "v_mov_b64 v[196:197], 0\n\t" \
        "v_mov_b64 v[198:199], 0\n\t" \
        "v_mov_b64 v[200:201], 0\n\t" \
        "v_mov_b64 v[202:203], 0\n\t" \
        "s_mov_b32 %0, 0\n\t" \
        "s_mov_b32 %1, %67\n\t" \
        "L_dl_%=:\n\t" \
        "s_sub_u32 %1, %1, 1\n\t" \
        "s_cbranch_scc0 L_dl_%=\n\t" \
        "s_barrier\n\t" \
        "L_pm_%=:\n\t" \
        "buffer_load_dwordx4 v[160:163], %52, %65, %68 offen sc1\n\t" \
        "buffer_load_dwordx4 v[164:167], %53, %65, %68 offen sc1\n\t" \
        "s_waitcnt vmcnt(0)\n\t" \
        "L_pw_%=:\n\t" \
        "ds_write_b128 %54, v[160:163]\n\t" \
        "v_max3_u32 v228, v160, v161, v162\n\t" \
        "v_max3_u32 v229, v163, v164, v165\n\t" \
        "ds_write_b128 %54, v[164:167] offset:1024\n\t" \
        "v_max3_u32 v228, v228, v166, v167\n\t" \
        "v_max_u32 v228, v228, v229\n\t" \
        "ds_write_b128 %54, v[160:163] offset:7168\n\t" \
        "v_cmp_ne_u32 vcc, -1, v228\n\t" \
        "ds_write_b128 %54, v[164:167] offset:8192\n\t" \
        "s_cmp_eq_u64 vcc, exec\n\t" \
        "s_cbranch_scc1 L_pmok_%=\n\t" \
        "s_add_u32 %0, %0, 1\n\t" \
        "s_sleep 1\n\t" \
        "s_cmp_lt_u32 %0, 131072\n\t" \
        "s_cbranch_scc1 L_pm_%=\n\t" \
        "L_pmok_%=:\n\t" \
        "v_smfmac_f32_16x16x64_bf16 v[188:191], v[160:163], %7, %50\n\t" \
        "buffer_load_dwordx4 a[144:147], %76, %65, %72 offen offset:0 sc1\n\t" \
        "v_smfmac_f32_16x16x64_bf16 v[192:195], v[160:163], %14, %50\n\t" \
        "buffer_load_dwordx4 a[148:151], %76, %65, %72 offen offset:1024 sc1\n\t" \
        "s_waitcnt lgkmcnt(0)\n\t" \
        "s_barrier\n\t" \
        "ds_read_b128 v[168:171], %54 offset:2048\n\t" \
        "ds_read_b128 v[172:175], %54 offset:3072\n\t" \
        "ds_read_b128 v[176:179], %54 offset:4096\n\t" \
        "ds_read_b128 v[180:183], %54 offset:5120\n\t" \
        "ds_read_b128 v[184:187], %54 offset:6144\n\t" \
        "v_smfmac_f32_16x16x64_bf16 v[188:191], v[164:167], %8, %50\n\t" \
        "buffer_load_dwordx4 a[200:203], %79, %65, %72 offen offset:2048 sc1\n\t" \
        "s_add_u32 %1, %66, 1\n\t" \
        "v_smfmac_f32_16x16x64_bf16 v[192:195], v[164:167], %15, %50\n\t" \
        "buffer_load_dwordx4 a[204:207], %79, %65, %72 offen offset:3072 sc1\n\t" \
        "v_mov_b32 v159, %1\n\t" \
        "s_waitcnt lgkmcnt(4)\n\t" \
        "v_smfmac_f32_16x16x64_bf16 v[188:191], v[168:171], %9, %50\n\t" \
        "buffer_load_dwordx4 a[152:155], %73, %65, %72 offen offset:0 sc1\n\t" \
        "v_smfmac_f32_16x16x64_bf16 v[192:195], v[168:171], %16, %50\n\t" \
        "buffer_load_dwordx4 a[156:159], %73, %65, %72 offen offset:1024 sc1\n\t" \
        "s_waitcnt lgkmcnt(3)\n\t" \
        "v_smfmac_f32_16x16x64_bf16 v[188:191], v[172:175], %10, %50\n\t" \
        "buffer_load_dwordx4 a[208:211], %76, %65, %72 offen offset:2048 sc1\n\t" \
        "v_smfmac_f32_16x16x64_bf16 v[192:195], v[172:175], %17, %50\n\t" \
        "buffer_load_dwordx4 a[212:215], %76, %65, %72 offen offset:3072 sc1\n\t" \
        "s_waitcnt lgkmcnt(2)\n\t" \
        "v_smfmac_f32_16x16x64_bf16 v[188:191], v[176:179], %11, %50\n\t" \
        "buffer_load_dwordx4 a[160:163], %73, %65, %72 offen offset:2048 sc1\n\t" \
        "v_smfmac_f32_16x16x64_bf16 v[192:195], v[176:179], %18, %50\n\t" \
        "buffer_load_dwordx4 a[164:167], %73, %65, %72 offen offset:3072 sc1\n\t" \
        "s_waitcnt lgkmcnt(1)\n\t" \
        "v_smfmac_f32_16x16x64_bf16 v[188:191], v[180:183], %12, %50\n\t" \
        "buffer_load_dwordx4 a[216:219], %77, %65, %72 offen offset:0 sc1\n\t" \
        "v_smfmac_f32_16x16x64_bf16 v[192:195], v[180:183], %19, %50\n\t" \
        "buffer_load_dwordx4 a[220:223], %77, %65, %72 offen offset:1024 sc1\n\t" \
        "s_waitcnt lgkmcnt(0)\n\t" \
        "v_smfmac_f32_16x16x64_bf16 v[188:191], v[184:187], %13, %50\n\t" \
        "buffer_load_dwordx4 a[168:171], %74, %65, %72 offen offset:0 sc1\n\t" \
        "v_smfmac_f32_16x16x64_bf16 v[192:195], v[184:187], %20, %50\n\t" \
        "buffer_load_dwordx4 a[172:175], %74, %65, %72 offen offset:1024 sc1\n\t" \
        "ds_write_b32 %55, v159\n\t" \
        "buffer_load_dwordx4 a[224:227], %77, %65, %72 offen offset:2048 sc1\n\t" \
        "s_nop 2\n\t" \
        "v_cvt_pk_bf16_f32 v228, v188, v189\n\t" \
        "v_cvt_pk_bf16_f32 v229, v190, v191\n\t" \
        "buffer_load_dwordx4 a[228:231], %77, %65, %72 offen offset:3072 sc1\n\t" \
        "s_nop 0\n\t" \
        "v_cvt_pk_bf16_f32 v230, v192, v193\n\t" \
        "v_cvt_pk_bf16_f32 v231, v194, v195\n\t" \
        "buffer_load_dwordx4 a[176:179], %74, %65, %72 offen offset:2048 sc1\n\t" \
        "s_nop 0\n\t" \
        "v_mfma_f32_16x16x32_bf16 v[236:239], v[228:231], %49, 0\n\t" \
        "buffer_load_dwordx4 a[180:183], %74, %65, %72 offen offset:3072 sc1\n\t" \
        "buffer_load_dwordx4 a[232:235], %78, %65, %72 offen offset:0 sc1\n\t" \
        "s_nop 5\n\t" \
        "v_max3_f32 v240, v236, v237, v238\n\t" \
        "v_max_f32 v240, v240, v239\n\t" \
        "v_mov_b32 v241, v240\n\t" \
        "buffer_load_dwordx4 a[236:239], %78, %65, %72 offen offset:1024 sc1\n\t" \
        "s_nop 0\n\t" \
        "v_permlane32_swap_b32 v240, v241\n\t" \
        "v_max_f32 v240, v240, v241\n\t" \
        "v_mov_b32 v241, v240\n\t" \
        "buffer_load_dwordx4 a[184:187], %75, %65, %72 offen offset:0 sc1\n\t" \
        "s_nop 0\n\t" \
        "v_permlane16_swap_b32 v240, v241\n\t" \
        "v_max_f32 v240, v240, v241\n\t" \
        "buffer_load_dwordx4 a[188:191], %75, %65, %72 offen offset:1024 sc1\n\t" \
        "s_nop 0\n\t" \
        "v_max_f32_dpp v240, v240, v240 row_ror:8 row_mask:0xf bank_mask:0xf\n\t" \
        "v_pk_add_f32 v[236:237], v[236:237], v[240:241] op_sel_hi:[1,0] neg_lo:[0,1] neg_hi:[0,1]\n\t" \
        "v_pk_add_f32 v[238:239], v[238:239], v[240:241] op_sel_hi:[1,0] neg_lo:[0,1] neg_hi:[0,1]\n\t" \
        "v_exp_f32 v236, v236\n\t" \
        "v_exp_f32 v237, v237\n\t" \
        "v_exp_f32 v238, v238\n\t" \
        "v_exp_f32 v239, v239\n\t" \
        "s_nop 0\n\t" \
        "v_pk_add_f32 v[242:243], v[236:237], v[238:239]\n\t" \
        "v_add_f32 v242, v242, v243\n\t" \
        "v_mov_b32 v241, v242\n\t" \
        "buffer_load_dwordx4 a[240:243], %78, %65, %72 offen offset:2048 sc1\n\t" \
        "s_nop 0\n\t" \
        "v_permlane32_swap_b32 v242, v241\n\t" \
        "v_add_f32 v242, v242, v241\n\t" \
        "v_mov_b32 v241, v242\n\t" \
        "buffer_load_dwordx4 a[244:247], %78, %65, %72 offen offset:3072 sc1\n\t" \
        "s_nop 0\n\t" \
        "v_permlane16_swap_b32 v242, v241\n\t" \
        "v_add_f32 v242, v242, v241\n\t" \
        "buffer_load_dwordx4 a[192:195], %75, %65, %72 offen offset:2048 sc1\n\t" \
        "s_nop 0\n\t" \
        "v_add_f32_dpp v242, v242, v242 row_ror:8 row_mask:0xf bank_mask:0xf\n\t" \
        "v_rcp_f32 v244, v242\n\t" \
        "v_log_f32 v246, v242\n\t" \
        "v_pk_mul_f32 v[236:237], v[236:237], v[244:245] op_sel_hi:[1,0]\n\t" \
        "v_pk_mul_f32 v[238:239], v[238:239], v[244:245] op_sel_hi:[1,0]\n\t" \
        "v_add_f32 v246, v246, v240\n\t" \
        "v_cvt_pk_bf16_f32 v228, v236, v237\n\t" \
        "v_cvt_pk_bf16_f32 v229, v238, v239\n\t" \
        "v_min_u32 v228, -2, v228\n\t" \
        "v_min_u32 v229, -2, v229\n\t" \
        "buffer_store_dwordx2 v[228:229], %58, %65, %69 offen sc1\n\t" \
        "v_min_u32 v246, -2, v246\n\t" \
        "buffer_store_dword v246, %56, %65, %69 offen sc1\n\t" \
        "buffer_load_dwordx4 a[196:199], %75, %65, %72 offen offset:3072 sc1\n\t" \
        "buffer_load_dwordx4 a[248:251], %79, %65, %72 offen offset:0 sc1\n\t" \
        "buffer_load_dwordx4 a[252:255], %79, %65, %72 offen offset:1024 sc1\n\t" \
        "s_xor_b32 %1, %69, 0x100000\n\t" \
        "buffer_store_dwordx4 %64, %57, %65, %1 offen sc1\n\t" \
        "v_mov_b64 v[236:237], 0\n\t" \
        "v_mov_b64 v[238:239], 0\n\t" \
        "v_mov_b64 v[240:241], 0\n\t" \
        "v_mov_b64 v[242:243], 0\n\t" \
        "v_mov_b64 v[244:245], 0\n\t" \
        "v_mov_b64 v[246:247], 0\n\t" \
        "v_mov_b64 v[248:249], 0\n\t" \
        "v_mov_b64 v[250:251], 0\n\t" \
        "s_nop 0\n\t" \
        "v_smfmac_f32_16x16x64_bf16 v[236:239], v[160:163], %21, %50\n\t" \
        "v_smfmac_f32_16x16x64_bf16 v[240:243], v[160:163], %28, %50\n\t" \
        "v_smfmac_f32_16x16x64_bf16 v[244:247], v[160:163], %35, %50\n\t" \
        "v_smfmac_f32_16x16x64_bf16 v[248:251], v[160:163], %42, %50\n\t" \
        "v_smfmac_f32_16x16x64_bf16 v[236:239], v[164:167], %22, %50\n\t" \
        "v_smfmac_f32_16x16x64_bf16 v[240:243], v[164:167], %29, %50\n\t" \
        "v_smfmac_f32_16x16x64_bf16 v[244:247], v[164:167], %36, %50\n\t" \
        "v_smfmac_f32_16x16x64_bf16 v[248:251], v[164:167], %43, %50\n\t" \
        "v_smfmac_f32_16x16x64_bf16 v[236:239], v[168:171], %23, %50\n\t" \
        "v_smfmac_f32_16x16x64_bf16 v[240:243], v[168:171], %30, %50\n\t" \
        "v_smfmac_f32_16x16x64_bf16 v[244:247], v[168:171], %37, %50\n\t" \
        "v_smfmac_f32_16x16x64_bf16 v[248:251], v[168:171], %44, %50\n\t" \
        "v_smfmac_f32_16x16x64_bf16 v[236:239], v[172:175], %24, %50\n\t" \
        "v_smfmac_f32_16x16x64_bf16 v[240:243], v[172:175], %31, %50\n\t" \
        "v_smfmac_f32_16x16x64_bf16 v[244:247], v[172:175], %38, %50\n\t" \
        "v_smfmac_f32_16x16x64_bf16 v[248:251], v[172:175], %45, %50\n\t" \
        "v_smfmac_f32_16x16x64_bf16 v[236:239], v[176:179], %25, %50\n\t" \
        "v_smfmac_f32_16x16x64_bf16 v[240:243], v[176:179], %32, %50\n\t" \
        "v_smfmac_f32_16x16x64_bf16 v[244:247], v[176:179], %39, %50\n\t" \
        "v_smfmac_f32_16x16x64_bf16 v[248:251], v[176:179], %46, %50\n\t" \
        "v_smfmac_f32_16x16x64_bf16 v[236:239], v[180:183], %26, %50\n\t" \
        "v_smfmac_f32_16x16x64_bf16 v[240:243], v[180:183], %33, %50\n\t" \
        "v_smfmac_f32_16x16x64_bf16 v[244:247], v[180:183], %40, %50\n\t" \
        "v_smfmac_f32_16x16x64_bf16 v[248:251], v[180:183], %47, %50\n\t" \
        "s_barrier\n\t" \
        "buffer_load_dwordx4 v[220:223], %60, %65, %70 offen sc1\n\t" \
        "buffer_load_dwordx4 v[224:227], %60, %65, %70 offen offset:1792 sc1\n\t" \
        "buffer_load_dwordx2 v[232:233], %61, %65, %70 offen sc1\n\t" \
        "buffer_load_dwordx2 v[228:229], %61, %65, %70 offen offset:128 sc1\n\t" \
        "buffer_load_dword v230, %62, %65, %70 offen sc1\n\t" \
        "v_smfmac_f32_16x16x64_bf16 v[236:239], v[184:187], %27, %50\n\t" \
        "v_smfmac_f32_16x16x64_bf16 v[240:243], v[184:187], %34, %50\n\t" \
        "v_smfmac_f32_16x16x64_bf16 v[244:247], v[184:187], %41, %50\n\t" \
        "v_smfmac_f32_16x16x64_bf16 v[248:251], v[184:187], %48, %50\n\t" \
        "s_nop 7\n\t" \
        "v_cvt_pk_bf16_f32 v188, v236, v237\n\t" \
        "v_cvt_pk_bf16_f32 v189, v238, v239\n\t" \
        "v_cvt_pk_bf16_f32 v192, v240, v241\n\t" \
        "v_cvt_pk_bf16_f32 v193, v242, v243\n\t" \
        "v_cvt_pk_bf16_f32 v190, v244, v245\n\t" \
        "v_cvt_pk_bf16_f32 v191, v246, v247\n\t" \
        "v_cvt_pk_bf16_f32 v194, v248, v249\n\t" \
        "v_cvt_pk_bf16_f32 v195, v250, v251\n\t" \
        "s_waitcnt vmcnt(0)\n\t" \
        "L_chk_%=:\n\t" \
        "v_mfma_f32_16x16x32_bf16 v[196:199], %2, v[220:223], 0\n\t" \
        "v_max3_u32 v212, v220, v221, v222\n\t" \
        "v_max3_u32 v213, v223, v224, v225\n\t" \
        "v_max3_u32 v212, v212, v226, v227\n\t" \
        "v_mfma_f32_16x16x32_bf16 v[200:203], %3, v[220:223], 0\n\t" \
        "v_max3_u32 v213, v213, v232, v233\n\t" \
        "v_max3_u32 v212, v212, v228, v229\n\t" \
        "v_max3_u32 v212, v212, v213, v230\n\t" \
        "v_mfma_f32_16x16x32_bf16 v[204:207], %4, v[224:227], 0\n\t" \
        "v_cmp_ne_u32 vcc, -1, v212\n\t" \
        "v_lshlrev_b32 v236, 16, v232\n\t" \
        "s_cmp_eq_u64 vcc, exec\n\t" \
        "s_cbranch_scc1 L_ok_%=\n\t" \
        "s_add_u32 %0, %0, 1\n\t" \
        "s_cmp_lt_u32 %0, 131072\n\t" \
        "s_cbranch_scc0 L_ok_%=\n\t" \
        "s_sleep 1\n\t" \
        "buffer_load_dwordx4 v[220:223], %60, %65, %70 offen sc1\n\t" \
        "buffer_load_dwordx4 v[224:227], %60, %65, %70 offen offset:1792 sc1\n\t" \
        "buffer_load_dwordx2 v[232:233], %61, %65, %70 offen sc1\n\t" \
        "buffer_load_dwordx2 v[228:229], %61, %65, %70 offen offset:128 sc1\n\t" \
        "buffer_load_dword v230, %62, %65, %70 offen sc1\n\t" \
        "s_waitcnt vmcnt(0)\n\t" \
        "s_branch L_chk_%=\n\t" \
        "L_ok_%=:\n\t" \
        "v_mfma_f32_16x16x32_bf16 v[208:211], %5, v[224:227], 0\n\t" \
        "v_and_b32 v237, 0xffff0000, v232\n\t" \
        "v_lshlrev_b32 v238, 16, v233\n\t" \
        "v_and_b32 v239, 0xffff0000, v233\n\t" \
        "v_lshlrev_b32 v240, 16, v228\n\t" \
        "v_and_b32 v241, 0xffff0000, v228\n\t" \
        "v_lshlrev_b32 v242, 16, v229\n\t" \
        "v_and_b32 v243, 0xffff0000, v229\n\t" \
        "v_pk_add_f32 v[236:237], v[236:237], v[240:241]\n\t" \
        "v_pk_add_f32 v[238:239], v[238:239], v[242:243]\n\t" \
        "v_max3_f32 v244, v236, v237, v230\n\t" \
        "v_max3_f32 v244, v244, v238, v239\n\t" \
        "v_mov_b32 v245, v244\n\t" \
        "s_nop 1\n\t" \
        "v_permlane16_swap_b32 v244, v245\n\t" \
        "v_max_f32 v244, v244, v245\n\t" \
        "v_pk_add_f32 v[236:237], v[236:237], v[244:245] op_sel_hi:[1,0] neg_lo:[0,1] neg_hi:[0,1]\n\t" \
        "v_pk_add_f32 v[238:239], v[238:239], v[244:245] op_sel_hi:[1,0] neg_lo:[0,1] neg_hi:[0,1]\n\t" \
        "v_sub_f32 v246, v230, v244\n\t" \
        "v_exp_f32 v236, v236\n\t" \
        "v_exp_f32 v237, v237\n\t" \
        "v_exp_f32 v238, v238\n\t" \
        "v_exp_f32 v239, v239\n\t" \
        "v_exp_f32 v246, v246\n\t" \
        "v_pk_add_f32 v[248:249], v[236:237], v[238:239]\n\t" \
        "v_add_f32 v248, v248, v249\n\t" \
        "v_mov_b32 v245, v248\n\t" \
        "buffer_load_dwordx4 %2, %51, %65, %71 offen sc1\n\t" \
        "s_add_u32 %1, %71, 57344\n\t" \
        "v_permlane16_swap_b32 v248, v245\n\t" \
        "v_add_f32 v248, v248, v245\n\t" \
        "v_add_f32 v248, v248, v246\n\t" \
        "v_rcp_f32 v250, v248\n\t" \
        "buffer_load_dwordx4 %3, %63, %65, %71 offen sc1\n\t" \
        "v_mul_f32 v240, v246, v250\n\t" \
        "v_mul_f32 v242, v246, v250\n\t" \
        "v_pk_mul_f32 v[236:237], v[236:237], v[250:251] op_sel_hi:[1,0]\n\t" \
        "v_pk_mul_f32 v[238:239], v[238:239], v[250:251] op_sel_hi:[1,0]\n\t" \
        "v_permlane32_swap_b32 v240, v242\n\t" \
        "v_cvt_pk_bf16_f32 v220, v236, v237\n\t" \
        "v_cvt_pk_bf16_f32 v222, v236, v237\n\t" \
        "v_cvt_pk_bf16_f32 v221, v238, v239\n\t" \
        "v_cvt_pk_bf16_f32 v223, v238, v239\n\t" \
        "v_pk_mul_f32 v[196:197], v[196:197], v[240:241] op_sel_hi:[1,0]\n\t" \
        "v_pk_mul_f32 v[198:199], v[198:199], v[240:241] op_sel_hi:[1,0]\n\t" \
        "v_permlane32_swap_b32 v220, v222\n\t" \
        "v_permlane32_swap_b32 v221, v223\n\t" \
        "v_pk_fma_f32 v[196:197], v[204:205], v[242:243], v[196:197] op_sel_hi:[1,0,1]\n\t" \
        "v_pk_fma_f32 v[198:199], v[206:207], v[242:243], v[198:199] op_sel_hi:[1,0,1]\n\t" \
        "v_pk_mul_f32 v[200:201], v[200:201], v[240:241] op_sel_hi:[1,0]\n\t" \
        "v_pk_mul_f32 v[202:203], v[202:203], v[240:241] op_sel_hi:[1,0]\n\t" \
        "v_mfma_f32_16x16x32_bf16 v[196:199], v[188:191], v[220:223], v[196:199]\n\t" \
        "v_pk_fma_f32 v[200:201], v[208:209], v[242:243], v[200:201] op_sel_hi:[1,0,1]\n\t" \
        "v_pk_fma_f32 v[202:203], v[210:211], v[242:243], v[202:203] op_sel_hi:[1,0,1]\n\t" \
        "buffer_load_dwordx4 %4, %51, %65, %1 offen sc1\n\t" \
        "buffer_load_dwordx4 %5, %63, %65, %1 offen sc1\n\t" \
        "v_mfma_f32_16x16x32_bf16 v[200:203], v[192:195], v[220:223], v[200:203]\n\t" \
        "s_nop 1\n\t" \
        "s_mov_b32 exec_lo, 0x00ff00ff\n\t" \
        "s_mov_b32 exec_hi, 0x00ff00ff\n\t" \
        "ds_write_b128 %59, v[196:199] offset:1024\n\t" \
        "s_not_b64 exec, exec\n\t" \
        "s_nop 1\n\t" \
        "ds_write_b128 %59, v[200:203] offset:1024\n\t" \
        "s_mov_b64 exec, -1\n\t" \
        "ds_write_b32 %55, v159 offset:16\n\t" \
        "s_nop 1\n\t" \
        : "=&s"(cnt), "=&s"(st), "+v"(ux[0]), "+v"(ux[1]), "+v"(ux[2]), "+v"(ux[3]), "+v"(dmy), "+{a[144:151]}"(wa[0][0]), "+{a[152:159]}"(wa[0][1]), "+{a[160:167]}"(wa[0][2]), "+{a[168:175]}"(wa[0][3]), "+{a[176:183]}"(wa[0][4]), "+{a[184:191]}"(wa[0][5]), "+{a[192:199]}"(wa[0][6]), "+{a[200:207]}"(wa[1][0]), "+{a[208:215]}"(wa[1][1]), "+{a[216:223]}"(wa[1][2]), "+{a[224:231]}"(wa[1][3]), "+{a[232:239]}"(wa[1][4]), "+{a[240:247]}"(wa[1][5]), "+{a[248:255]}"(wa[1][6]) \
        : "a"(wb[0][0]), "a"(wb[0][1]), "a"(wb[0][2]), "a"(wb[0][3]), "a"(wb[0][4]), "a"(wb[0][5]), "a"(wb[0][6]), "a"(wb[1][0]), "a"(wb[1][1]), "a"(wb[1][2]), "a"(wb[1][3]), "a"(wb[1][4]), "a"(wb[1][5]), "a"(wb[1][6]), "a"(wb[2][0]), "a"(wb[2][1]), "a"(wb[2][2]), "a"(wb[2][3]), "v"(wb[2][4]), "v"(wb[2][5]), "v"(wb[2][6]), "v"(wb[3][0]), "v"(wb[3][1]), "v"(wb[3][2]), "v"(wb[3][3]), "v"(wb[3][4]), "v"(wb[3][5]), "v"(wb[3][6]), "v"(bsel), "v"(sidx), "v"(gL16), "v"(gM0), "v"(gM1), "v"(aMr), "v"(aFl), "v"(gLs), "v"(gC1), "v"(gPub), "v"(aW), "v"(gE), "v"(gS0), "v"(gS1), "v"(gU1), "v"(ones), "s"(rs), "s"(s), "s"(dly), "s"(sM), "s"(sW), "s"(sR), "s"(sUX), "s"(sHX), "v"(gG[0]), "v"(gG[1]), "v"(gG[2]), "v"(gG[3]), "v"(gG[4]), "v"(gG[5]), "v"(gG[6]) \
        : "v159", "v160", "v161", "v162", "v163", "v164", "v165", "v166", "v167", "v168", "v169", "v170", "v171", "v172", "v173", "v174", "v175", "v176", "v177", "v178", "v179", "v180", "v181", "v182", "v183", "v184", "v185", "v186", "v187", "v188", "v189", "v190", "v191", "v192", "v193", "v194", "v195", "v196", "v197", "v198", "v199", "v200", "v201", "v202", "v203", "v204", "v205", "v206", "v207", "v208", "v209", "v210", "v211", "v212", "v213", "v214", "v215", "v216", "v217", "v218", "v219", "v220", "v221", "v222", "v223", "v224", "v225", "v226", "v227", "v228", "v229", "v230", "v231", "v232", "v233", "v234", "v235", "v236", "v237", "v238", "v239", "v240", "v241", "v242", "v243", "v244", "v245", "v246", "v247", "v248", "v249", "v250", "v251", "v252", "v253", "v254", "v255", "vcc", "scc", "memory");

#define REP_BLOCK_STEP() \
    asm volatile( \
        "v_mov_b64 v[188:189], 0\n\t" \
        "v_mov_b64 v[190:191], 0\n\t" \
        "v_mov_b64 v[192:193], 0\n\t" \
        "v_mov_b64 v[194:195], 0\n\t" \
        "v_mov_b64 v[196:197], 0\n\t" \
        "v_mov_b64 v[198:199], 0\n\t" \
        "v_mov_b64 v[200:201], 0\n\t" \
        "v_mov_b64 v[202:203], 0\n\t" \
        "s_mov_b32 %0, 0\n\t" \
        "s_sleep 8\n\t" \
        "L_pm_%=:\n\t" \
        "buffer_load_dwordx4 v[160:163], %54, %63, %66 offen sc1\n\t" \
        "buffer_load_dwordx4 v[164:167], %55, %63, %66 offen sc1\n\t" \
        "s_waitcnt vmcnt(0)\n\t" \
        "L_pw_%=:\n\t" \
        "ds_write_b128 %56, v[160:163]\n\t" \
        "v_max3_u32 v228, v160, v161, v162\n\t" \
        "v_max3_u32 v229, v163, v164, v165\n\t" \
        "ds_write_b128 %56, v[164:167] offset:1024\n\t" \
        "v_max3_u32 v228, v228, v166, v167\n\t" \
        "v_max_u32 v228, v228, v229\n\t" \
        "ds_write_b128 %56, v[160:163] offset:7168\n\t" \
        "v_cmp_ne_u32 vcc, -1, v228\n\t" \
        "ds_write_b128 %56, v[164:167] offset:8192\n\t" \
        "s_cmp_eq_u64 vcc, exec\n\t" \
        "s_cbranch_scc1 L_pmok_%=\n\t" \
        "s_add_u32 %0, %0, 1\n\t" \
        "s_sleep 4\n\t" \
        "s_cmp_lt_u32 %0, 131072\n\t" \
        "s_cbranch_scc1 L_pm_%=\n\t" \
        "L_pmok_%=:\n\t" \
        "v_smfmac_f32_16x16x64_bf16 v[188:191], v[160:163], %6, %51\n\t" \
        "v_smfmac_f32_16x16x64_bf16 v[192:195], v[160:163], %13, %51\n\t" \
        "v_smfmac_f32_16x16x64_bf16 v[196:199], v[160:163], %20, %51\n\t" \
        "v_smfmac_f32_16x16x64_bf16 v[200:203], v[160:163], %27, %51\n\t" \
        "s_waitcnt lgkmcnt(0)\n\t" \
        "s_barrier\n\t" \
        "ds_read_b128 v[168:171], %56 offset:2048\n\t" \
        "ds_read_b128 v[172:175], %56 offset:3072\n\t" \
        "ds_read_b128 v[176:179], %56 offset:4096\n\t" \
        "ds_read_b128 v[180:183], %56 offset:5120\n\t" \
        "ds_read_b128 v[184:187], %56 offset:6144\n\t" \
        "v_smfmac_f32_16x16x64_bf16 v[188:191], v[164:167], %7, %51\n\t" \
        "s_add_u32 %1, %64, 1\n\t" \
        "v_smfmac_f32_16x16x64_bf16 v[192:195], v[164:167], %14, %51\n\t" \
        "v_mov_b32 v159, %1\n\t" \
        "v_smfmac_f32_16x16x64_bf16 v[196:199], v[164:167], %21, %51\n\t" \
        "buffer_store_dword v159, %62, %63, 0 offen sc1\n\t" \
        "v_smfmac_f32_16x16x64_bf16 v[200:203], v[164:167], %28, %51\n\t" \
        "s_waitcnt lgkmcnt(4)\n\t" \
        "v_smfmac_f32_16x16x64_bf16 v[188:191], v[168:171], %8, %51\n\t" \
        "v_smfmac_f32_16x16x64_bf16 v[192:195], v[168:171], %15, %51\n\t" \
        "v_smfmac_f32_16x16x64_bf16 v[196:199], v[168:171], %22, %51\n\t" \
        "v_smfmac_f32_16x16x64_bf16 v[200:203], v[168:171], %29, %51\n\t" \
        "s_waitcnt lgkmcnt(3)\n\t" \
        "v_smfmac_f32_16x16x64_bf16 v[188:191], v[172:175], %9, %51\n\t" \
        "v_smfmac_f32_16x16x64_bf16 v[192:195], v[172:175], %16, %51\n\t" \
        "v_smfmac_f32_16x16x64_bf16 v[196:199], v[172:175], %23, %51\n\t" \
        "v_smfmac_f32_16x16x64_bf16 v[200:203], v[172:175], %30, %51\n\t" \
        "s_waitcnt lgkmcnt(2)\n\t" \
        "v_smfmac_f32_16x16x64_bf16 v[188:191], v[176:179], %10, %51\n\t" \
        "v_smfmac_f32_16x16x64_bf16 v[192:195], v[176:179], %17, %51\n\t" \
        "s_waitcnt lgkmcnt(1)\n\t" \
        "v_smfmac_f32_16x16x64_bf16 v[188:191], v[180:183], %11, %51\n\t" \
        "v_smfmac_f32_16x16x64_bf16 v[192:195], v[180:183], %18, %51\n\t" \
        "s_waitcnt lgkmcnt(0)\n\t" \
        "v_smfmac_f32_16x16x64_bf16 v[188:191], v[184:187], %12, %51\n\t" \
        "v_smfmac_f32_16x16x64_bf16 v[192:195], v[184:187], %19, %51\n\t" \
        "v_smfmac_f32_16x16x64_bf16 v[196:199], v[176:179], %24, %51\n\t" \
        "v_smfmac_f32_16x16x64_bf16 v[200:203], v[176:179], %31, %51\n\t" \
        "v_smfmac_f32_16x16x64_bf16 v[196:199], v[180:183], %25, %51\n\t" \
        "v_smfmac_f32_16x16x64_bf16 v[200:203], v[180:183], %32, %51\n\t" \
        "v_smfmac_f32_16x16x64_bf16 v[196:199], v[184:187], %26, %51\n\t" \
        "v_smfmac_f32_16x16x64_bf16 v[200:203], v[184:187], %33, %51\n\t" \
        "s_nop 0\n\t" \
        "v_cvt_pk_bf16_f32 v228, v188, v189\n\t" \
        "v_cvt_pk_bf16_f32 v229, v190, v191\n\t" \
        "v_cvt_pk_bf16_f32 v230, v192, v193\n\t" \
        "v_cvt_pk_bf16_f32 v231, v194, v195\n\t" \
        "s_nop 1\n\t" \
        "v_mfma_f32_16x16x32_bf16 v[236:239], v[228:231], %50, 0\n\t" \
        "v_cvt_pk_bf16_f32 v232, v196, v197\n\t" \
        "v_cvt_pk_bf16_f32 v233, v198, v199\n\t" \
        "v_cvt_pk_bf16_f32 v234, v200, v201\n\t" \
        "v_cvt_pk_bf16_f32 v235, v202, v203\n\t" \
        "s_nop 1\n\t" \
        "v_mfma_f32_16x16x32_bf16 v[240:243], v[232:235], %50, 0\n\t" \
        "s_nop 0\n\t" \
        "v_cvt_pk_bf16_f32 v252, v236, v237\n\t" \
        "v_cvt_pk_bf16_f32 v253, v238, v239\n\t" \
        "s_nop 4\n\t" \
        "v_cvt_pk_bf16_f32 v254, v240, v241\n\t" \
        "v_cvt_pk_bf16_f32 v255, v242, v243\n\t" \
        "s_nop 1\n\t" \
        "v_mfma_f32_16x16x32_bf16 v[220:223], v[252:255], %2, 0\n\t" \
        "v_mfma_f32_16x16x32_bf16 v[224:227], v[252:255], %3, 0\n\t" \
        "v_mfma_f32_16x16x32_bf16 v[228:231], v[252:255], %4, 0\n\t" \
        "v_mfma_f32_16x16x32_bf16 v[232:235], v[252:255], %5, 0\n\t" \
        "buffer_load_dwordx4 %2, %52, %63, %67 offen offset:0 sc1\n\t" \
        "buffer_load_dwordx4 %3, %52, %63, %67 offen offset:1024 sc1\n\t" \
        "buffer_load_dwordx4 %4, %53, %63, %67 offen offset:0 sc1\n\t" \
        "buffer_load_dwordx4 %5, %53, %63, %67 offen offset:1024 sc1\n\t" \
        "s_nop 3\n\t" \
        "s_mov_b32 exec_hi, 0\n\t" \
        "ds_write_b128 %57, v[220:223] offset:2048\n\t" \
        "ds_write_b128 %57, v[228:231] offset:3072\n\t" \
        "s_not_b64 exec, exec\n\t" \
        "ds_write_b128 %57, v[224:227] offset:2048\n\t" \
        "ds_write_b128 %57, v[232:235] offset:3072\n\t" \
        "s_mov_b64 exec, -1\n\t" \
        "ds_read_b128 v[188:191], %59 offset:0\n\t" \
        "ds_read_b128 v[192:195], %59 offset:1024\n\t" \
        "ds_read_b128 v[196:199], %59 offset:2048\n\t" \
        "ds_read_b128 v[200:203], %59 offset:3072\n\t" \
        "ds_read_b128 v[204:207], %59 offset:4096\n\t" \
        "ds_read_b128 v[208:211], %59 offset:5120\n\t" \
        "ds_read_b128 v[212:215], %59 offset:6144\n\t" \
        "ds_read_b128 v[216:219], %59 offset:7168\n\t" \
        "v_mov_b64 v[236:237], 0\n\t" \
        "v_mov_b64 v[238:239], 0\n\t" \
        "v_mov_b64 v[240:241], 0\n\t" \
        "v_mov_b64 v[242:243], 0\n\t" \
        "v_mov_b64 v[244:245], 0\n\t" \
        "v_mov_b64 v[246:247], 0\n\t" \
        "v_mov_b64 v[248:249], 0\n\t" \
        "v_mov_b64 v[250:251], 0\n\t" \
        "s_nop 0\n\t" \
        "v_smfmac_f32_16x16x64_bf16 v[236:239], v[160:163], %34, %51\n\t" \
        "v_smfmac_f32_16x16x64_bf16 v[240:243], v[160:163], %41, %51\n\t" \
        "v_smfmac_f32_16x16x64_bf16 v[236:239], v[164:167], %35, %51\n\t" \
        "v_smfmac_f32_16x16x64_bf16 v[240:243], v[164:167], %42, %51\n\t" \
        "v_smfmac_f32_16x16x64_bf16 v[244:247], v[160:163], %48, %51\n\t" \
        "s_waitcnt lgkmcnt(6)\n\t" \
        "v_smfmac_f32_16x16x64_bf16 v[248:251], v[160:163], v[188:195], %51\n\t" \
        "ds_read_b128 v[188:191], %59 offset:8192\n\t" \
        "ds_read_b128 v[192:195], %59 offset:9216\n\t" \
        "v_smfmac_f32_16x16x64_bf16 v[244:247], v[164:167], %49, %51\n\t" \
        "s_waitcnt lgkmcnt(6)\n\t" \
        "v_smfmac_f32_16x16x64_bf16 v[248:251], v[164:167], v[196:203], %51\n\t" \
        "ds_read_b128 v[196:199], %59 offset:10240\n\t" \
        "ds_read_b128 v[200:203], %59 offset:11264\n\t" \
        "v_smfmac_f32_16x16x64_bf16 v[236:239], v[168:171], %36, %51\n\t" \
        "s_waitcnt lgkmcnt(6)\n\t" \
        "v_smfmac_f32_16x16x64_bf16 v[244:247], v[168:171], v[204:211], %51\n\t" \
        "ds_read_b128 v[204:207], %59 offset:12288\n\t" \
        "ds_read_b128 v[208:211], %59 offset:13312\n\t" \
        "v_smfmac_f32_16x16x64_bf16 v[240:243], v[168:171], %43, %51\n\t" \
        "s_waitcnt lgkmcnt(6)\n\t" \
        "v_smfmac_f32_16x16x64_bf16 v[248:251], v[168:171], v[212:219], %51\n\t" \
        "ds_read_b128 v[212:215], %59 offset:14336\n\t" \
        "ds_read_b128 v[216:219], %59 offset:15360\n\t" \
        "v_smfmac_f32_16x16x64_bf16 v[236:239], v[172:175], %37, %51\n\t" \
        "s_waitcnt lgkmcnt(6)\n\t" \
        "v_smfmac_f32_16x16x64_bf16 v[244:247], v[172:175], v[188:195], %51\n\t" \
        "ds_read_b128 v[188:191], %59 offset:16384\n\t" \
        "ds_read_b128 v[192:195], %59 offset:17408\n\t" \
        "v_smfmac_f32_16x16x64_bf16 v[240:243], v[172:175], %44, %51\n\t" \
        "s_waitcnt lgkmcnt(6)\n\t" \
        "v_smfmac_f32_16x16x64_bf16 v[248:251], v[172:175], v[196:203], %51\n\t" \
        "ds_read_b128 v[196:199], %59 offset:18432\n\t" \
        "ds_read_b128 v[200:203], %59 offset:19456\n\t" \
        "v_smfmac_f32_16x16x64_bf16 v[236:239], v[176:179], %38, %51\n\t" \
        "s_waitcnt lgkmcnt(6)\n\t" \
        "v_smfmac_f32_16x16x64_bf16 v[244:247], v[176:179], v[204:211], %51\n\t" \
        "ds_read_b128 v[204:207], %59 offset:20480\n\t" \
        "ds_read_b128 v[208:211], %59 offset:21504\n\t" \
        "v_smfmac_f32_16x16x64_bf16 v[240:243], v[176:179], %45, %51\n\t" \
        "s_waitcnt lgkmcnt(6)\n\t" \
        "v_smfmac_f32_16x16x64_bf16 v[248:251], v[176:179], v[212:219], %51\n\t" \
        "ds_read_b128 v[212:215], %59 offset:22528\n\t" \
        "ds_read_b128 v[216:219], %59 offset:23552\n\t" \
        "v_smfmac_f32_16x16x64_bf16 v[236:239], v[180:183], %39, %51\n\t" \
        "s_waitcnt lgkmcnt(6)\n\t" \
        "v_smfmac_f32_16x16x64_bf16 v[244:247], v[180:183], v[188:195], %51\n\t" \
        "v_smfmac_f32_16x16x64_bf16 v[240:243], v[180:183], %46, %51\n\t" \
        "s_waitcnt lgkmcnt(4)\n\t" \
        "v_smfmac_f32_16x16x64_bf16 v[248:251], v[180:183], v[196:203], %51\n\t" \
        "v_smfmac_f32_16x16x64_bf16 v[236:239], v[184:187], %40, %51\n\t" \
        "s_waitcnt lgkmcnt(2)\n\t" \
        "v_smfmac_f32_16x16x64_bf16 v[244:247], v[184:187], v[204:211], %51\n\t" \
        "v_smfmac_f32_16x16x64_bf16 v[240:243], v[184:187], %47, %51\n\t" \
        "s_waitcnt lgkmcnt(0)\n\t" \
        "v_smfmac_f32_16x16x64_bf16 v[248:251], v[184:187], v[212:219], %51\n\t" \
        "s_nop 7\n\t" \
        "v_cvt_pk_bf16_f32 v188, v236, v237\n\t" \
        "v_cvt_pk_bf16_f32 v189, v238, v239\n\t" \
        "v_cvt_pk_bf16_f32 v192, v240, v241\n\t" \
        "v_cvt_pk_bf16_f32 v193, v242, v243\n\t" \
        "v_cvt_pk_bf16_f32 v190, v244, v245\n\t" \
        "v_cvt_pk_bf16_f32 v191, v246, v247\n\t" \
        "v_cvt_pk_bf16_f32 v194, v248, v249\n\t" \
        "v_cvt_pk_bf16_f32 v195, v250, v251\n\t" \
        "v_min_u32 v188, -2, v188\n\t" \
        "v_min_u32 v189, -2, v189\n\t" \
        "v_min_u32 v190, -2, v190\n\t" \
        "v_min_u32 v191, -2, v191\n\t" \
        "v_min_u32 v192, -2, v192\n\t" \
        "v_min_u32 v193, -2, v193\n\t" \
        "v_min_u32 v194, -2, v194\n\t" \
        "v_min_u32 v195, -2, v195\n\t" \
        "buffer_store_dwordx4 v[188:191], %60, %63, %68 offen sc1\n\t" \
        "buffer_store_dwordx4 v[192:195], %60, %63, %68 offen offset:1024 sc1\n\t" \
        "s_waitcnt lgkmcnt(0)\n\t" \
        "s_barrier\n\t" \
        "s_sub_u32 %1, %65, 1\n\t" \
        "s_cmp_gt_u32 %1, 1\n\t" \
        "s_cbranch_scc1 L_end_%=\n\t" \
        "s_mov_b32 exec_hi, 0\n\t" \
        "ds_read_b128 v[188:191], %58 offset:0\n\t" \
        "ds_read_b128 v[192:195], %58 offset:512\n\t" \
        "ds_read_b128 v[196:199], %58 offset:4096\n\t" \
        "ds_read_b128 v[200:203], %58 offset:4608\n\t" \
        "ds_read_b128 v[204:207], %58 offset:8192\n\t" \
        "ds_read_b128 v[208:211], %58 offset:8704\n\t" \
        "ds_read_b128 v[212:215], %58 offset:12288\n\t" \
        "ds_read_b128 v[216:219], %58 offset:12800\n\t" \
        "s_waitcnt lgkmcnt(6)\n\t" \
        "v_pk_add_f32 v[188:189], v[188:189], v[192:193]\n\t" \
        "v_pk_add_f32 v[190:191], v[190:191], v[194:195]\n\t" \
        "s_waitcnt lgkmcnt(4)\n\t" \
        "v_pk_add_f32 v[196:197], v[196:197], v[200:201]\n\t" \
        "v_pk_add_f32 v[198:199], v[198:199], v[202:203]\n\t" \
        "v_pk_add_f32 v[188:189], v[188:189], v[196:197]\n\t" \
        "v_pk_add_f32 v[190:191], v[190:191], v[198:199]\n\t" \
        "s_waitcnt lgkmcnt(2)\n\t" \
        "v_pk_add_f32 v[204:205], v[204:205], v[208:209]\n\t" \
        "v_pk_add_f32 v[206:207], v[206:207], v[210:211]\n\t" \
        "v_pk_add_f32 v[188:189], v[188:189], v[204:205]\n\t" \
        "v_pk_add_f32 v[190:191], v[190:191], v[206:207]\n\t" \
        "s_waitcnt lgkmcnt(0)\n\t" \
        "v_pk_add_f32 v[212:213], v[212:213], v[216:217]\n\t" \
        "v_pk_add_f32 v[214:215], v[214:215], v[218:219]\n\t" \
        "v_pk_add_f32 v[188:189], v[188:189], v[212:213]\n\t" \
        "v_pk_add_f32 v[190:191], v[190:191], v[214:215]\n\t" \
        "v_cvt_pk_bf16_f32 v220, v188, v189\n\t" \
        "v_cvt_pk_bf16_f32 v221, v190, v191\n\t" \
        "v_min_u32 v220, -2, v220\n\t" \
        "v_min_u32 v221, -2, v221\n\t" \
        "buffer_store_dwordx2 v[220:221], %61, %63, %69 offen sc1\n\t" \
        "s_mov_b64 exec, -1\n\t" \
        "L_end_%=:\n\t" \
        "s_nop 1\n\t" \
        : "=&s"(cnt), "=&s"(st), "+v"(ux[0]), "+v"(ux[1]), "+v"(ux[2]), "+v"(ux[3]) \
        : "a"(wa[0][0]), "a"(wa[0][1]), "a"(wa[0][2]), "a"(wa[0][3]), "a"(wa[0][4]), "a"(wa[0][5]), "a"(wa[0][6]), "a"(wa[1][0]), "a"(wa[1][1]), "a"(wa[1][2]), "a"(wa[1][3]), "a"(wa[1][4]), "a"(wa[1][5]), "a"(wa[1][6]), "a"(wa[2][0]), "a"(wa[2][1]), "a"(wa[2][2]), "a"(wa[2][3]), "a"(wa[2][4]), "a"(wa[2][5]), "a"(wa[2][6]), "a"(wa[3][0]), "a"(wa[3][1]), "a"(wa[3][2]), "a"(wa[3][3]), "a"(wa[3][4]), "a"(wa[3][5]), "a"(wa[3][6]), "a"(wb[0][0]), "a"(wb[0][1]), "a"(wb[0][2]), "a"(wb[0][3]), "v"(wb[0][4]), "v"(wb[0][5]), "v"(wb[0][6]), "v"(wb[1][0]), "v"(wb[1][1]), "v"(wb[1][2]), "v"(wb[1][3]), "v"(wb[1][4]), "v"(wb[1][5]), "v"(wb[1][6]), "v"(wb[2][0]), "v"(wb[2][1]), "v"(bsel), "v"(sidx), "v"(gL16), "v"(gH1), "v"(gM0), "v"(gM1), "v"(aMr), "v"(aW), "v"(aRB), "v"(aWB), "v"(gUm), "v"(gPM), "v"(gFl), "s"(rs), "s"(s), "s"(wave), "s"(sM), "s"(sHX), "s"(sUMF), "s"(sSXM) \
        : "v159", "v160", "v161", "v162", "v163", "v164", "v165", "v166", "v167", "v168", "v169", "v170", "v171", "v172", "v173", "v174", "v175", "v176", "v177", "v178", "v179", "v180", "v181", "v182", "v183", "v184", "v185", "v186", "v187", "v188", "v189", "v190", "v191", "v192", "v193", "v194", "v195", "v196", "v197", "v198", "v199", "v200", "v201", "v202", "v203", "v204", "v205", "v206", "v207", "v208", "v209", "v210", "v211", "v212", "v213", "v214", "v215", "v216", "v217", "v218", "v219", "v220", "v221", "v222", "v223", "v224", "v225", "v226", "v227", "v228", "v229", "v230", "v231", "v232", "v233", "v234", "v235", "v236", "v237", "v238", "v239", "v240", "v241", "v242", "v243", "v244", "v245", "v246", "v247", "v248", "v249", "v250", "v251", "v252", "v253", "v254", "v255", "vcc", "scc", "memory");

#define REP_UM_STEP() \
    asm volatile( \
        "v_mov_b64 v[188:189], 0\n\t" \
        "v_mov_b64 v[190:191], 0\n\t" \
        "v_mov_b64 v[192:193], 0\n\t" \
        "v_mov_b64 v[194:195], 0\n\t" \
        "v_mov_b64 v[196:197], 0\n\t" \
        "v_mov_b64 v[198:199], 0\n\t" \
        "v_mov_b64 v[200:201], 0\n\t" \
        "v_mov_b64 v[202:203], 0\n\t" \
        "s_mov_b32 %0, 0\n\t" \
        "s_sleep 8\n\t" \
        "L_pm_%=:\n\t" \
        "buffer_load_dwordx4 v[160:163], %31, %35, %37 offen sc1\n\t" \
        "buffer_load_dwordx4 v[164:167], %32, %35, %37 offen sc1\n\t" \
        "s_waitcnt vmcnt(0)\n\t" \
        "L_pw_%=:\n\t" \
        "ds_write_b128 %33, v[160:163]\n\t" \
        "v_max3_u32 v228, v160, v161, v162\n\t" \
        "v_max3_u32 v229, v163, v164, v165\n\t" \
        "ds_write_b128 %33, v[164:167] offset:1024\n\t" \
        "v_max3_u32 v228, v228, v166, v167\n\t" \
        "v_max_u32 v228, v228, v229\n\t" \
        "ds_write_b128 %33, v[160:163] offset:7168\n\t" \
        "v_cmp_ne_u32 vcc, -1, v228\n\t" \
        "ds_write_b128 %33, v[164:167] offset:8192\n\t" \
        "s_cmp_eq_u64 vcc, exec\n\t" \
        "s_cbranch_scc1 L_pmok_%=\n\t" \
        "s_add_u32 %0, %0, 1\n\t" \
        "s_sleep 4\n\t" \
        "s_cmp_lt_u32 %0, 131072\n\t" \
        "s_cbranch_scc1 L_pm_%=\n\t" \
        "L_pmok_%=:\n\t" \
        "s_waitcnt lgkmcnt(0)\n\t" \
        "s_barrier\n\t" \
        "ds_read_b128 v[168:171], %33 offset:2048\n\t" \
        "ds_read_b128 v[172:175], %33 offset:3072\n\t" \
        "ds_read_b128 v[176:179], %33 offset:4096\n\t" \
        "ds_read_b128 v[180:183], %33 offset:5120\n\t" \
        "ds_read_b128 v[184:187], %33 offset:6144\n\t" \
        "s_waitcnt lgkmcnt(0)\n\t" \
        "v_mov_b64 v[236:237], 0\n\t" \
        "v_mov_b64 v[238:239], 0\n\t" \
        "v_mov_b64 v[240:241], 0\n\t" \
        "v_mov_b64 v[242:243], 0\n\t" \
        "v_mov_b64 v[244:245], 0\n\t" \
        "v_mov_b64 v[246:247], 0\n\t" \
        "v_mov_b64 v[248:249], 0\n\t" \
        "v_mov_b64 v[250:251], 0\n\t" \
        "s_nop 0\n\t" \
        "v_smfmac_f32_16x16x64_bf16 v[236:239], v[160:163], %2, %30\n\t" \
        "v_smfmac_f32_16x16x64_bf16 v[240:243], v[160:163], %9, %30\n\t" \
        "v_smfmac_f32_16x16x64_bf16 v[244:247], v[160:163], %16, %30\n\t" \
        "v_smfmac_f32_16x16x64_bf16 v[248:251], v[160:163], %23, %30\n\t" \
        "v_smfmac_f32_16x16x64_bf16 v[236:239], v[164:167], %3, %30\n\t" \
        "v_smfmac_f32_16x16x64_bf16 v[240:243], v[164:167], %10, %30\n\t" \
        "v_smfmac_f32_16x16x64_bf16 v[244:247], v[164:167], %17, %30\n\t" \
        "v_smfmac_f32_16x16x64_bf16 v[248:251], v[164:167], %24, %30\n\t" \
        "v_smfmac_f32_16x16x64_bf16 v[236:239], v[168:171], %4, %30\n\t" \
        "v_smfmac_f32_16x16x64_bf16 v[240:243], v[168:171], %11, %30\n\t" \
        "v_smfmac_f32_16x16x64_bf16 v[244:247], v[168:171], %18, %30\n\t" \
        "v_smfmac_f32_16x16x64_bf16 v[248:251], v[168:171], %25, %30\n\t" \
        "v_smfmac_f32_16x16x64_bf16 v[236:239], v[172:175], %5, %30\n\t" \
        "v_smfmac_f32_16x16x64_bf16 v[240:243], v[172:175], %12, %30\n\t" \
        "v_smfmac_f32_16x16x64_bf16 v[244:247], v[172:175], %19, %30\n\t" \
        "v_smfmac_f32_16x16x64_bf16 v[248:251], v[172:175], %26, %30\n\t" \
        "v_smfmac_f32_16x16x64_bf16 v[236:239], v[176:179], %6, %30\n\t" \
        "v_smfmac_f32_16x16x64_bf16 v[240:243], v[176:179], %13, %30\n\t" \
        "v_smfmac_f32_16x16x64_bf16 v[244:247], v[176:179], %20, %30\n\t" \
        "v_smfmac_f32_16x16x64_bf16 v[248:251], v[176:179], %27, %30\n\t" \
        "v_smfmac_f32_16x16x64_bf16 v[236:239], v[180:183], %7, %30\n\t" \
        "v_smfmac_f32_16x16x64_bf16 v[240:243], v[180:183], %14, %30\n\t" \
        "v_smfmac_f32_16x16x64_bf16 v[244:247], v[180:183], %21, %30\n\t" \
        "v_smfmac_f32_16x16x64_bf16 v[248:251], v[180:183], %28, %30\n\t" \
        "v_smfmac_f32_16x16x64_bf16 v[236:239], v[184:187], %8, %30\n\t" \
        "v_smfmac_f32_16x16x64_bf16 v[240:243], v[184:187], %15, %30\n\t" \
        "v_smfmac_f32_16x16x64_bf16 v[244:247], v[184:187], %22, %30\n\t" \
        "v_smfmac_f32_16x16x64_bf16 v[248:251], v[184:187], %29, %30\n\t" \
        "s_nop 7\n\t" \
        "v_cvt_pk_bf16_f32 v188, v236, v237\n\t" \
        "v_cvt_pk_bf16_f32 v189, v238, v239\n\t" \
        "v_cvt_pk_bf16_f32 v192, v240, v241\n\t" \
        "v_cvt_pk_bf16_f32 v193, v242, v243\n\t" \
        "v_cvt_pk_bf16_f32 v190, v244, v245\n\t" \
        "v_cvt_pk_bf16_f32 v191, v246, v247\n\t" \
        "v_cvt_pk_bf16_f32 v194, v248, v249\n\t" \
        "v_cvt_pk_bf16_f32 v195, v250, v251\n\t" \
        "v_min_u32 v188, -2, v188\n\t" \
        "v_min_u32 v189, -2, v189\n\t" \
        "v_min_u32 v190, -2, v190\n\t" \
        "v_min_u32 v191, -2, v191\n\t" \
        "v_min_u32 v192, -2, v192\n\t" \
        "v_min_u32 v193, -2, v193\n\t" \
        "v_min_u32 v194, -2, v194\n\t" \
        "v_min_u32 v195, -2, v195\n\t" \
        "buffer_store_dwordx4 v[188:191], %34, %35, %38 offen sc1\n\t" \
        "buffer_store_dwordx4 v[192:195], %34, %35, %38 offen offset:1024 sc1\n\t" \
        "s_waitcnt lgkmcnt(0)\n\t" \
        "s_barrier\n\t" \
        "s_nop 1\n\t" \
        : "=&s"(cnt), "=&s"(st) \
        : "a"(wb[0][0]), "a"(wb[0][1]), "a"(wb[0][2]), "a"(wb[0][3]), "a"(wb[0][4]), "a"(wb[0][5]), "a"(wb[0][6]), "a"(wb[1][0]), "a"(wb[1][1]), "a"(wb[1][2]), "a"(wb[1][3]), "a"(wb[1][4]), "a"(wb[1][5]), "a"(wb[1][6]), "a"(wb[2][0]), "a"(wb[2][1]), "a"(wb[2][2]), "a"(wb[2][3]), "a"(wb[2][4]), "a"(wb[2][5]), "a"(wb[2][6]), "a"(wb[3][0]), "a"(wb[3][1]), "a"(wb[3][2]), "a"(wb[3][3]), "a"(wb[3][4]), "a"(wb[3][5]), "a"(wb[3][6]), "v"(sidx), "v"(gM0), "v"(gM1), "v"(aMr), "v"(gUm), "s"(rs), "s"(s), "s"(sM), "s"(sUMF) \
        : "v159", "v160", "v161", "v162", "v163", "v164", "v165", "v166", "v167", "v168", "v169", "v170", "v171", "v172", "v173", "v174", "v175", "v176", "v177", "v178", "v179", "v180", "v181", "v182", "v183", "v184", "v185", "v186", "v187", "v188", "v189", "v190", "v191", "v192", "v193", "v194", "v195", "v196", "v197", "v198", "v199", "v200", "v201", "v202", "v203", "v204", "v205", "v206", "v207", "v208", "v209", "v210", "v211", "v212", "v213", "v214", "v215", "v216", "v217", "v218", "v219", "v220", "v221", "v222", "v223", "v224", "v225", "v226", "v227", "v228", "v229", "v230", "v231", "v232", "v233", "v234", "v235", "v236", "v237", "v238", "v239", "v240", "v241", "v242", "v243", "v244", "v245", "v246", "v247", "v248", "v249", "v250", "v251", "v252", "v253", "v254", "v255", "vcc", "scc", "memory");

template <bool SMX, bool REP>
DI void rec_loop(const Params& p, const int r, unsigned char* lds, const int wave_) {
    const int lane = lane_id(), tid = wave_ * 64 + lane;
    const int n16 = lane & 15, g = lane >> 4, i8 = lane & 7, hi8 = (lane >> 3) & 1;
    const int h = r >> 1;
    const unsigned hf = r & 1, wave = wave_;
    const int blk = SMX ? 0 : hf * 4 + wave;
    const bool val = r < 28;
    const int ct = val ? r : r - 28;
    const unsigned oob = val ? 0u : 0x80000000u;
    unsigned* flags = (unsigned*)(p.ws + OFF_FLAGS);
    const __amdgpu_buffer_rsrc_t rc = __builtin_amdgcn_make_buffer_rsrc(p.ws, 0, (int)WS_END, 0x00020000);
    v4u rs;
    rs.x = __builtin_amdgcn_readfirstlane((unsigned)(uintptr_t)p.ws); rs.y = __builtin_amdgcn_readfirstlane((unsigned)((uintptr_t)p.ws >> 32)); rs.z = (unsigned)WS_END; rs.w = 0x00020000u;
    const int sidx = hi8 ? 0xEEEE : 0x4444;
    v4u bsel;
    {
        const unsigned one = 0x3F80u;
        unsigned e[8];
#pragma unroll
        for (int jj = 0; jj < 8; ++jj) e[jj] = (((jj >> 2) == hi8) && (((4 * g + (jj & 3)) & 7) == i8)) ? one : 0u;
        bsel = (v4u){e[0] | (e[1] << 16), e[2] | (e[3] << 16), e[4] | (e[5] << 16), e[6] | (e[7] << 16)};
    }
    if (REP) {
        unsigned spins = 0;
        while ((unsigned)__builtin_amdgcn_readfirstlane(__hip_atomic_load(flags + FL_GU + 16 * 32, __ATOMIC_RELAXED, __HIP_MEMORY_SCOPE_AGENT)) < 56u) {
            __builtin_amdgcn_s_sleep(32);
            if (spin_fail(spins, flags)) break;
        }
    }
    constexpr int NT = SMX ? 2 : 4;
    v8u wa[NT][7], wb[4][7];
    v4u ux[4];
    auto rot = [&](int j) { const int k = 2 * (int)wave + j; return k >= 7 ? k - 7 : k; };
    const unsigned char* wbsrc = p.ws + OFF_WB + ((size_t)(4 * wave) * 28 + ct) * 14336 + lane * 16;
    if (!SMX) {
#pragma unroll
        for (int half = 0; half < 2; ++half) {
            v4u tmp[12];
#pragma unroll
            for (int jj = 0; jj < 6; ++jj) {
                const int j = half * 6 + jj, q = j < 2 ? 3 : 2 + (j & 1), kb = rot(j < 2 ? j : 2 + ((j - 2) >> 1));
                tmp[2 * jj] = *(const v4u*)(wbsrc + (size_t)q * 28 * 14336 + kb * 2048);
                tmp[2 * jj + 1] = *(const v4u*)(wbsrc + (size_t)q * 28 * 14336 + kb * 2048 + 1024);
            }
#pragma unroll
            for (int k = 0; k < 12; ++k) *(v4u*)(lds + L_WB + wave * 24576 + (half * 12 + k) * 1024 + lane * 16) = tmp[k];
        }
    } else {
        *(v4u*)(lds + L_RED + wave * RSTR + lane * 16) = (v4u){0, 0, 0, 0};
#pragma unroll
        for (int k = 2; k < 4; ++k) *(v4u*)(lds + L_RED + wave * RSTR + k * 1024 + lane * 16) = (v4u){0, 0, 0, 0};
    }
#pragma unroll
    for (int q = 0; q < 4; ++q)
#pragma unroll
        for (int kb = 0; kb < 7; ++kb)
            if (SMX || q < 2 || (q == 2 && kb < 2)) wb[q][kb] = cat8(*(const v4u*)(wbsrc + (size_t)q * 28 * 14336 + rot(kb) * 2048), *(const v4u*)(wbsrc + (size_t)q * 28 * 14336 + rot(kb) * 2048 + 1024));
    if (!SMX) {
        const unsigned char* src = p.ws + OFF_WAR + ((size_t)h * 7 + blk) * 4 * 14336 + lane * 16;
#pragma unroll
        for (int t = 0; t < NT; ++t)
#pragma unroll
            for (int kb = 0; kb < 7; ++kb) wa[t][kb] = cat8(*(const v4u*)(src + (t * 14 + rot(kb) * 2) * 1024), *(const v4u*)(src + (t * 14 + rot(kb) * 2 + 1) * 1024));
    }
    unsigned* pmin = (unsigned*)(p.ws + OFF_COMM + chunk_off(3, 1, 0));
    if (!REP && SMX && r < 16) {
        unsigned mn[4] = {0xFFFFFFFFu, 0xFFFFFFFFu, 0xFFFFFFFFu, 0xFFFFFFFFu};
#pragma unroll 1
        for (int rep = 0; rep < NPROBE; ++rep) {
#pragma unroll
            for (int c = 0; c < 4; ++c) {
                const unsigned char* pa = p.ws + OFF_COMM + chunk_off(3, 0, c) + (rep & 1) * 4096 + ((r >> 1) * 4 + (rep >> 1)) * 128;
                unsigned long long ta, tb; unsigned x;
                asm volatile("s_memtime %0\n\ts_waitcnt lgkmcnt(0)\n\ts_load_dword %2, %3, 0x0\n\ts_waitcnt lgkmcnt(0)\n\ts_memtime %1\n\ts_waitcnt lgkmcnt(0)"
                             : "=&s"(ta), "=&s"(tb), "=&s"(x) : "s"(pa) : "memory");
                mn[c] = min(mn[c], RFL((unsigned)(tb - ta)));
            }
        }
        if (lane < 4) __hip_atomic_fetch_min(pmin + lane, lane == 0 ? mn[0] : (lane == 1 ? mn[1] : (lane == 2 ? mn[2] : mn[3])), __ATOMIC_RELAXED, __HIP_MEMORY_SCOPE_AGENT);
        asm volatile("s_waitcnt vmcnt(0)" ::: "memory");
        if (lane == 0) __hip_atomic_fetch_add(flags + FL_SLOT, 1u, __ATOMIC_RELAXED, __HIP_MEMORY_SCOPE_AGENT);
    }
    auto wait_cnt = [&](int idx0, int n, unsigned want) {
        unsigned spins = 0;
        for (;;) {
            const unsigned v = __hip_atomic_load(flags + idx0 + min(lane, n - 1) * 32, __ATOMIC_RELAXED, __HIP_MEMORY_SCOPE_AGENT);
            if (__all(v >= want)) return RFL(v);
            __builtin_amdgcn_s_sleep(4);
            if (spin_fail(spins, flags)) return 1u;
        }
    };
    if (tid < 8) *(unsigned*)(lds + L_LSE + tid * 4) = 0u;
    const unsigned ldsb = (unsigned)(uintptr_t)(LAS unsigned char*)lds;
    const unsigned gL16 = lane * 16;
    const unsigned gM0 = wave * 2048 + lane * 16, gM1 = (wave == 3) ? lane * 16 : gM0 + 1024;
    const unsigned aMr = ldsb + L_MEM + wave * 2048 + lane * 16;
    const unsigned aW = ldsb + L_RED + wave * RSTR + lane * 16;
    const unsigned aWB = ldsb + L_WB + wave * 24576 + lane * 16;
    bool dead = false;
    wait_vm0();
    if (REP) {
        const unsigned gH1 = lane * 16 + 14336;
        const unsigned aRB = ldsb + L_RED + 2048 + ((wave - 1) & 1) * 1024 + lane * 16;
        const unsigned gUm = (lane * 16) | oob;
        const unsigned gPM = ((wave - 1) & 1) * 256 + n16 * 16 + (g & 1) * 8;
        const unsigned gFl = (wave == 1 && lane < 32) ? (unsigned)OFF_FLAGS + (FL_REC + r * 32 + lane) * 4 : 0x80000000u;
        const unsigned bHX = (unsigned)OFF_HN + (unsigned)(2 * blk) * 1024u;
        wait_cnt(FL_H, 16, 8u);
        if (!SMX) {
#pragma unroll
            for (int q = 0; q < 4; ++q) ux[q] = ld16(rc, bHX + (q >> 1) * 14336 + (q & 1) * 1024 + lane * 16);
        }
        wait_vm0();
        __syncthreads();
#pragma unroll 1
        for (int s_ = 0; s_ < NSEG - 1; ++s_) {
            const unsigned s = RFL(s_);
            const unsigned sM = RFL((unsigned)OFF_HIST + s * 7168u), sHX = RFL(bHX + (s + 1) * 28672u);
            const unsigned sUMF = RFL((unsigned)OFF_UMF + ((s * 28 + ct) * 4 + wave) * 2048u), sSXM = RFL((unsigned)OFF_SXM + ((s * NH + h) * 2 + hf) * 512u);
            unsigned cnt, st;
            if (SMX) { REP_UM_STEP(); } else { REP_BLOCK_STEP(); }
            __builtin_amdgcn_sched_barrier(0);
            if (RFL(cnt) >= POLL_LIM) { dead = true; break; }
        }
        if (!SMX) asm volatile("s_waitcnt vmcnt(0)" : "+v"(ux[0]), "+v"(ux[1]), "+v"(ux[2]), "+v"(ux[3]) :: "memory");
        else asm volatile("s_waitcnt vmcnt(0)" ::: "memory");
        __syncthreads();
        if (tid < 32) __hip_atomic_store(flags + FL_REC + r * 32 + tid, (unsigned)NSEG + 1u, __ATOMIC_RELAXED, __HIP_MEMORY_SCOPE_AGENT);
    } else {
        const unsigned bUX = (unsigned)OFF_U + (unsigned)(4 * wave * 28 + ct) * 1024u;
        wait_cnt(FL_GU + 16 * 32, 3, 56u);
        if (SMX) {
            const unsigned o = (unsigned)OFF_G + (unsigned)(h * 2) * 14336u + lane * 16;
#pragma unroll
            for (int t = 0; t < 2; ++t)
#pragma unroll
                for (int kb = 0; kb < 7; ++kb) wa[t][kb] = cat8(ld16(rc, o + (t * 14 + rot(kb) * 2) * 1024), ld16(rc, o + (t * 14 + rot(kb) * 2 + 1) * 1024));
        }
#pragma unroll
        for (int q = 0; q < 4; ++q) ux[q] = ld16(rc, bUX + q * 28672 + lane * 16);
        wait_cnt(FL_SLOT, 1, 8u);
        unsigned slot = 0;
        {
            const unsigned mv = __hip_atomic_load(pmin + (lane & 3), __ATOMIC_RELAXED, __HIP_MEMORY_SCOPE_AGENT);
            const unsigned m0 = __builtin_amdgcn_readlane(mv, 0), m1 = __builtin_amdgcn_readlane(mv, 1), m2 = __builtin_amdgcn_readlane(mv, 2), m3 = __builtin_amdgcn_readlane(mv, 3);
            const unsigned b01 = min(m0, m1), b23 = min(m2, m3);
            slot = b01 <= b23 ? (m0 <= m1 ? 0u : 1u) : (m2 <= m3 ? 2u : 3u);
        }
        const unsigned gU1 = lane * 16 + 28672;
        unsigned gG[7];
#pragma unroll
        for (int i = 0; i < 7; ++i) gG[i] = lane * 16 + i * 4096;
        const unsigned aMb = ldsb + L_MEM + blk * 1024 + lane * 16;
        const unsigned aR1 = ldsb + L_RED + (((lane >> 3) & 1) * 16 + i8) * 16;
        const unsigned aRB = ldsb + L_RED + 1024 + lane * 16;
        const unsigned aFl = ldsb + L_LSE + wave * 4, aFr = ldsb + L_LSE;
        const unsigned gLs = lane < 32 ? (h & 7) * HREC + 768 + lane * 4 : 0x80000000u;
        const unsigned gPub = SMX ? (h & 7) * HREC + (g * 8 + i8) * 16 + hi8 * 8 : (h & 7) * HREC + 512 + hf * 128 + n16 * 8;
        const unsigned gE = ((wave & 1) * 4 + hi8) * HREC + (g * 8 + i8) * 16;
        const unsigned gS0 = ((wave & 1) * 4 + (g >> 1) * 2 + hi8) * HREC + 512 + ((g & 1) * 8 + i8) * 8;
        const unsigned gS1 = ((wave & 1) * 4 + (g >> 1) * 2 + hi8) * HREC + 768 + i8 * 4;
        const unsigned gPM = ((ct >> 2) * 1024 + (((ct & 1) * 2 + (g >> 1)) * 16 + ((ct >> 1) & 1) * 8 + i8) * 16 + (g & 1) * 8) | oob;
        const v4u ones = {0xFFFFFFFFu, 0xFFFFFFFFu, 0xFFFFFFFFu, 0xFFFFFFFFu};
        const unsigned gC1 = SMX ? (lane < 32 ? (h & 7) * HREC + lane * 16 : (lane < 40 ? (h & 7) * HREC + 768 + (lane - 32) * 16 : 0x80000000u))
                                 : (lane < 8 ? (h & 7) * HREC + 512 + hf * 128 + lane * 16 : 0x80000000u);
        const unsigned gC2 = (lane < 16 && val) ? (ct >> 2) * 1024 + (((ct & 1) * 2 + (lane >> 3)) * 16 + ((ct >> 1) & 1) * 8 + i8) * 16 : 0x80000000u;
        const unsigned dly = RFL((!SMX && wave == 0) ? D_M0 : 0);
        const unsigned pk = wave == 0 ? REC_PK0 : REC_PK;
        unsigned nextchk = 2, dmy = lane + 7;
        wait_vm0();
        __syncthreads();
#pragma unroll 1
        for (int s_ = 0; s_ < NSEG - 1; ++s_) {
            const unsigned s = RFL(s_);
            if (s == nextchk) {
                wait_cnt(s == 2 ? FL_GU + 19 * 32 : FL_GU + ((s + 1) >> 2) * 32, s == 2 ? 5 : (s == 7 ? 3 : (s == 19 ? 3 : 8)), 56u);
                nextchk = s == 2 ? 7 : (s == 7 ? 19 : (s == 19 ? 31 : 99));
                wait_vm0();
            }
            const unsigned sM = RFL(s == 0 ? 0u : chunk_off(0, s, slot)), sMn = RFL(chunk_off(0, s + 1, slot));
            const unsigned sX = chunk_off(1, s, slot);
            const unsigned sW = RFL(sX + ((unsigned)(h >> 3) << 15)), sR = RFL(sX + ((wave >> 1) << 15));
            const unsigned sUX = RFL(bUX + (s + REC_UXS) * (NH * 28 * 1024)), sHX = RFL((unsigned)OFF_G + (((s + 1) * NH + h) * 2) * 14336u);
            const unsigned sHc = RFL((r == HISTWG && s > 0) ? (unsigned)OFF_HIST + s * 7168u : 0u);
            unsigned cnt, st;
            if (SMX) { REC_SMX_STEP(); } else { REC_BLOCK_STEP(); }
            __builtin_amdgcn_sched_barrier(0);
            if (RFL(cnt) >= POLL_LIM) { dead = true; break; }
        }
        if (SMX) asm volatile("s_waitcnt vmcnt(0)" : "+v"(ux[0]), "+v"(ux[1]), "+v"(ux[2]), "+v"(ux[3]) :: "memory");
        __builtin_amdgcn_sched_barrier(0);
        if (!dead) {
            const unsigned gH1 = lane * 16 + 14336;
            const unsigned aRB = ldsb + L_RED + 2048 + ((wave - 1) & 1) * 1024 + lane * 16;
            const unsigned gUm = (lane * 16) | oob;
            const unsigned gPM = ((wave - 1) & 1) * 256 + n16 * 16 + (g & 1) * 8;
            const unsigned gFl = 0x80000000u;
            const unsigned s = RFL(NSEG - 1 + (lane & 0));
            const unsigned sHX = RFL((unsigned)OFF_HN + (unsigned)(2 * blk) * 1024u + s * 28672u);
            if (!SMX) {
                asm volatile("s_nop 4\n\tbuffer_load_dwordx4 %0, %4, %6, %7 offen sc1\n\tbuffer_load_dwordx4 %1, %4, %6, %7 offen offset:1024 sc1\n\t"
                             "buffer_load_dwordx4 %2, %5, %6, %7 offen sc1\n\tbuffer_load_dwordx4 %3, %5, %6, %7 offen offset:1024 sc1"
                             : "+v"(ux[0]), "+v"(ux[1]), "+v"(ux[2]), "+v"(ux[3]) : "v"(gL16), "v"(gH1), "s"(rs), "s"(sHX) : "memory");
            }
            const unsigned sM = RFL(chunk_off(0, s, slot));
            const unsigned sUMF = RFL((unsigned)OFF_UMF + ((s * 28 + ct) * 4 + wave) * 2048u), sSXM = RFL((unsigned)OFF_SXM + ((s * NH + h) * 2 + hf) * 512u);
            unsigned cnt, st;
            if (SMX) { REP_UM_STEP(); } else { REP_BLOCK_STEP(); }
            if (RFL(cnt) >= POLL_LIM) dead = true;
        }
        asm volatile("s_waitcnt vmcnt(0)" : "+v"(ux[0]), "+v"(ux[1]), "+v"(ux[2]), "+v"(ux[3]), "+v"(dmy) :: "memory");
    }
    if (dead) __hip_atomic_store(flags + FL_TMO, 1u, __ATOMIC_RELAXED, __HIP_MEMORY_SCOPE_AGENT);
}

template <bool REP>
DI void role_rec(const Params& p, int r, unsigned char* lds, int wave) {
    if ((r & 1) && wave == 3) rec_loop<true, REP>(p, r, lds, wave);
    else rec_loop<false, REP>(p, r, lds, wave);
}

constexpr int FL_E = 3328, FL_X1 = 3328 + 2048, FL_F = 3328 + 4096;
constexpr int T_RED = 0, T_H2 = 16384, T_MOD = T_H2 + 28672, T_ST = T_MOD + 4096, T_END = T_ST + 2048;
constexpr int NGRP = 12;
DI unsigned mxu(v4u v) { return max(max(v.x, v.y), max(v.z, v.w)); }
DI v4u nosent4(v4u v) { return (v4u){nosent(v.x), nosent(v.y), nosent(v.z), nosent(v.w)}; }

DI void signal_add(unsigned* c, int wave) {
    asm volatile("s_waitcnt vmcnt(0)" ::: "memory");
    __syncthreads();
    if (wave == 0 && lane_id() == 0) __hip_atomic_fetch_add(c, 1u, __ATOMIC_RELAXED, __HIP_MEMORY_SCOPE_AGENT);
}
DI void wait_ge(unsigned* c, unsigned want, unsigned* flags, int wave) {
    if (wave == 0) {
        unsigned spins = 0;
        while ((unsigned)__builtin_amdgcn_readfirstlane(__hip_atomic_load(c, __ATOMIC_RELAXED, __HIP_MEMORY_SCOPE_AGENT)) < want) {
            __builtin_amdgcn_s_sleep(2);
            if (spin_fail(spins, flags)) break;
        }
    }
    __syncthreads();
}
DI void wait_rec(unsigned* flags, unsigned want, int wave) {
    if (wave == 0) {
        unsigned spins = 0;
        for (;;) {
            const unsigned v = __hip_atomic_load(flags + FL_REC + (lane_id() & 31) * 32, __ATOMIC_RELAXED, __HIP_MEMORY_SCOPE_AGENT);
            if (__all(v >= want)) break;
            if (__any(v + 1u < want)) __builtin_amdgcn_s_sleep(64); else __builtin_amdgcn_s_sleep(6);
            if (spin_fail(spins, flags)) break;
        }
    }
    __syncthreads();
}
DI float sw16max(float v) { auto b = __builtin_amdgcn_permlane16_swap(__float_as_uint(v), __float_as_uint(v), false, false); return fmaxf(__uint_as_float(b[0]), __uint_as_float(b[1])); }
DI float sw16add(float v) { auto b = __builtin_amdgcn_permlane16_swap(__float_as_uint(v), __float_as_uint(v), false, false); return __uint_as_float(b[0]) + __uint_as_float(b[1]); }

constexpr int G_MOD = 114688, G_END = G_MOD + 4096;
#define MF_AV(acc, wa_, b_) asm("s_nop 1\n\tv_mfma_f32_16x16x32_bf16 %0, %1, %2, %0" : "+v"(acc) : "a"(wa_), "v"(b_))
#define MF_VA(acc, a_, wb_) asm("s_nop 1\n\tv_mfma_f32_16x16x32_bf16 %0, %1, %2, %0" : "+v"(acc) : "v"(a_), "a"(wb_))
#define MG_AV(acc, wa_, b_) asm("v_mfma_f32_16x16x32_bf16 %0, %1, %2, %0" : "+v"(acc) : "a"(wa_), "v"(b_))
#define MG_VA(acc, a_, wb_) asm("v_mfma_f32_16x16x32_bf16 %0, %1, %2, %0" : "+v"(acc) : "v"(a_), "a"(wb_))
#define ACC_INIT4(a, b, c, d) asm("s_nop 1" : "+v"(a), "+v"(b), "+v"(c), "+v"(d))
#define ACC_FENCE4(a, b, c, d) asm("s_nop 7" : "+v"(a), "+v"(b), "+v"(c), "+v"(d))
#define ACC_READY4(a, b, c, d) do {} while (0)
DI float row16sum(float v) {
    v += __uint_as_float(__builtin_amdgcn_update_dpp(0, __float_as_uint(v), 0x128, 0xf, 0xf, false));
    v += __uint_as_float(__builtin_amdgcn_update_dpp(0, __float_as_uint(v), 0x124, 0xf, 0xf, false));
    v += __uint_as_float(__builtin_amdgcn_update_dpp(0, __float_as_uint(v), 0x122, 0xf, 0xf, false));
    v += __uint_as_float(__builtin_amdgcn_update_dpp(0, __float_as_uint(v), 0x121, 0xf, 0xf, false));
    return v;
}
DI void role_ln(const Params& p, int tt, int wave) {
    const int lane = lane_id(), tid = wave * 64 + lane;
    const int j = lane & 15, tok = wave * 4 + (lane >> 4);
    const bool act = j < 14;
    const int jj = act ? j : 0, c0 = 64 * (jj >> 1) + 16 * (jj & 1);
    const __amdgpu_buffer_rsrc_t rc = __builtin_amdgcn_make_buffer_rsrc(p.ws, 0, (int)WS_END, 0x00020000);
    const float* mod = (const float*)(p.ws + OFF_MOD);
    const u16* xr = p.x + (size_t)(tt * 16 + tok) * DM + c0;
    v4u xv[4];
#pragma unroll
    for (int i = 0; i < 4; ++i) xv[i] = *(const v4u*)(xr + (i >> 1) * 32 + (i & 1) * 8);
    f32x4 sa[8], ca[8];
#pragma unroll
    for (int i = 0; i < 8; ++i) { sa[i] = *(const f32x4*)(mod + c0 + (i >> 2) * 32 + (i & 3) * 4); ca[i] = *(const f32x4*)(mod + DM + c0 + (i >> 2) * 32 + (i & 3) * 4); }
    float f[32];
#pragma unroll
    for (int i = 0; i < 4; ++i)
#pragma unroll
        for (int d = 0; d < 4; ++d) { f[i * 8 + 2 * d] = bflo(xv[i][d]); f[i * 8 + 2 * d + 1] = bfhi(xv[i][d]); }
    float sum = 0.f;
#pragma unroll
    for (int i = 0; i < 32; ++i) sum += f[i];
    const float mu = row16sum(act ? sum : 0.f) * (1.f / DM);
    float var = 0.f;
#pragma unroll
    for (int i = 0; i < 32; ++i) { f[i] -= mu; var += f[i] * f[i]; }
    const float rs = 1.f / sqrtf(row16sum(act ? var : 0.f) * (1.f / DM) + 1e-5f);
    unsigned o[16];
#pragma unroll
    for (int i = 0; i < 16; ++i) o[i] = pk2(f[2 * i] * rs * (1.f + ca[i >> 1][(2 * i) & 3]) + sa[i >> 1][(2 * i) & 3], f[2 * i + 1] * rs * (1.f + ca[i >> 1][(2 * i + 1) & 3]) + sa[i >> 1][(2 * i + 1) & 3]);
    if (act) {
#pragma unroll
        for (int g = 0; g < 4; ++g)
            st16(rc, (unsigned)OFF_HPI + (unsigned)(tt * 14 + jj) * 1024u + (g * 16 + tok) * 16, (v4u){o[2 * g], o[8 + 2 * g], o[2 * g + 1], o[8 + 2 * g + 1]});
#pragma unroll
        for (int i = 0; i < 4; ++i)
            st16(rc, (unsigned)OFF_HN + (unsigned)(tt * 14 + (jj >> 1) * 2 + (i >> 1)) * 1024u + ((2 * (jj & 1) + (i & 1)) * 16 + tok) * 16, (v4u){o[4 * i], o[4 * i + 1], o[4 * i + 2], o[4 * i + 3]});
    }
    signal_add((unsigned*)(p.ws + OFF_FLAGS) + FL_H + (tt >> 3) * 32, wave);
}

template <bool ISG>
DI void gemm_rounds(const Params& p, int gq, unsigned char* lds, int h, int cb, int wave) {
    const int lane = lane_id(), tid = wave * 64 + lane;
    const unsigned l16 = lane * 16;
    unsigned* flags = (unsigned*)(p.ws + OFF_FLAGS);
    const __amdgpu_buffer_rsrc_t rc = __builtin_amdgcn_make_buffer_rsrc(p.ws, 0, (int)WS_END, 0x00020000);
    const unsigned char* wsrc = p.ws + (ISG ? OFF_WAR : OFF_WB) + ((size_t)h * 28 + 4 * cb) * 14336;
    v4u wf[4][14];
    int woff[4];
#pragma unroll
    for (int t = 0; t < 4; ++t) {
        const int n16 = lane & 15, wl = (16 * t + n16) & 31, r4 = wl & 7;
        woff[t] = ISG ? ((t >> 1) + 2 * (r4 >> 2)) * 14336 + ((lane >> 4) * 16 + 4 * (wl >> 3) + (r4 & 3)) * 16 : t * 14336 + lane * 16;
    }
#pragma unroll
    for (int kk = 0; kk < 6; ++kk)
#pragma unroll
        for (int t = 0; t < 4; ++t) wf[t][kk] = *(const v4u*)(wsrc + woff[t] + kk * 1024);
    v4u pf[28];
    const int mt0 = gq == 2 ? 2 : 3 + gq;
    {
        unsigned spins = 0;
        for (;;) {
            const unsigned v = __hip_atomic_load(flags + FL_H + (lane & 15) * 32, __ATOMIC_RELAXED, __HIP_MEMORY_SCOPE_AGENT);
            if (__all(v >= 8u)) break;
            __builtin_amdgcn_s_sleep(2);
            if (spin_fail(spins, flags)) break;
        }
    }
#pragma unroll
    for (int i = 0; i < 7; ++i) pf[i] = ld16s(rc, l16, (unsigned)OFF_HPI + (unsigned)(gq * 28 + wave * 7 + i) * 1024u);
    __builtin_amdgcn_sched_barrier(0);
#pragma unroll
    for (int kk = 6; kk < 14; ++kk)
#pragma unroll
        for (int t = 0; t < 4; ++t) wf[t][kk] = *(const v4u*)(wsrc + woff[t] + kk * 1024);
#pragma unroll 1
    for (int sg = gq; sg < 8; sg += 3) {
#pragma unroll
        for (int i = 0; i < 7; ++i) *(v4u*)(lds + (wave * 7 + i) * 1024 + lane * 16) = pf[i];
        __syncthreads();
        {
            const int sn = sg + 3 < 8 ? sg + 3 : sg;
#pragma unroll
            for (int i = 0; i < 7; ++i) pf[i] = ld16s(rc, l16, (unsigned)OFF_HPI + (unsigned)(sn * 28 + wave * 7 + i) * 1024u);
        }
        v4u hf[2][14];
#pragma unroll
        for (int m = 0; m < 2; ++m)
#pragma unroll
            for (int kk = 0; kk < 14; ++kk) hf[m][kk] = *(const v4u*)(lds + (m * 14 + kk) * 1024 + lane * 16);
        f32x4 acc[2][4];
#pragma unroll
        for (int m = 0; m < 2; ++m)
#pragma unroll
            for (int t = 0; t < 4; ++t) acc[m][t] = (f32x4){0, 0, 0, 0};
#pragma unroll
        for (int m = 0; m < 2; ++m) ACC_INIT4(acc[m][0], acc[m][1], acc[m][2], acc[m][3]);
        __builtin_amdgcn_sched_barrier(0);
#pragma unroll
        for (int kk = 0; kk < 14; ++kk)
#pragma unroll
            for (int m = 0; m < 2; ++m)
#pragma unroll
                for (int t = 0; t < 4; ++t) { if (ISG) MG_AV(acc[m][t], wf[t][kk], hf[m][kk]); else MG_VA(acc[m][t], hf[m][kk], wf[t][kk]); }
#pragma unroll
        for (int m = 0; m < 2; ++m) ACC_FENCE4(acc[m][0], acc[m][1], acc[m][2], acc[m][3]);
        if (ISG) {
#pragma unroll
            for (int m = 0; m < 2; ++m)
#pragma unroll
                for (int u = 0; u < 2; ++u) {
                    const v4u o = {pk2(acc[m][u][0], acc[m][u][1]), pk2(acc[m][u + 2][0], acc[m][u + 2][1]), pk2(acc[m][u][2], acc[m][u][3]), pk2(acc[m][u + 2][2], acc[m][u + 2][3])};
                    st16s(rc, l16, (unsigned)OFF_G + (unsigned)((((sg * NH + h) * 2 + m) * 7 + cb) * 2 + u) * 1024u, o);
                }
        } else {
#pragma unroll
            for (int t = 0; t < 4; ++t) st16s(rc, l16, (unsigned)OFF_U + (unsigned)((sg * NH + h) * 28 + 4 * cb + t) * 1024u, pk8(acc[0][t], acc[1][t]));
        }
        signal_add(flags + FL_GU + 16 * 32 + sg * 32, wave);
    }
#pragma unroll
    for (int i = 0; i < 28; ++i) pf[i] = ld16s(rc, l16, (unsigned)OFF_HPI + (unsigned)(mt0 * 112 + wave * 28 + i) * 1024u);
#pragma unroll 1
    for (int rd = 0; rd < 5; ++rd) {
        const int mt = mt0 + 3 * rd;
        if (mt > 15) break;
#pragma unroll
        for (int i = 0; i < 28; ++i) *(v4u*)(lds + (wave * 28 + i) * 1024 + lane * 16) = pf[i];
        const int mn = mt + 3 <= 15 ? mt + 3 : mt;
        __syncthreads();
#pragma unroll
        for (int i = 0; i < 28; ++i) pf[i] = ld16s(rc, l16, (unsigned)OFF_HPI + (unsigned)(mn * 112 + wave * 28 + i) * 1024u);
#pragma unroll 1
        for (int ps = 0; ps < 2; ++ps) {
            f32x4 acc[4][4];
#pragma unroll
            for (int m = 0; m < 4; ++m)
#pragma unroll
                for (int t = 0; t < 4; ++t) acc[m][t] = (f32x4){0, 0, 0, 0};
#pragma unroll
            for (int m = 0; m < 4; ++m) ACC_INIT4(acc[m][0], acc[m][1], acc[m][2], acc[m][3]);
            const unsigned char* hl = lds + ps * 57344 + lane * 16;
            v4u hf[2][4];
#pragma unroll
            for (int m = 0; m < 4; ++m) hf[0][m] = *(const v4u*)(hl + (m * 14) * 1024);
#pragma unroll
            for (int kk = 0; kk < 14; ++kk) {
                if (kk < 13) {
#pragma unroll
                    for (int m = 0; m < 4; ++m) hf[(kk + 1) & 1][m] = *(const v4u*)(hl + (m * 14 + kk + 1) * 1024);
                }
                __builtin_amdgcn_sched_barrier(0);
#pragma unroll
                for (int m = 0; m < 4; ++m)
#pragma unroll
                    for (int t = 0; t < 4; ++t) { if (ISG) MG_AV(acc[m][t], wf[t][kk], hf[kk & 1][m]); else MG_VA(acc[m][t], hf[kk & 1][m], wf[t][kk]); }
                __builtin_amdgcn_sched_barrier(0);
            }
#pragma unroll
            for (int m = 0; m < 4; ++m) ACC_FENCE4(acc[m][0], acc[m][1], acc[m][2], acc[m][3]);
            if (ISG) {
#pragma unroll
                for (int m = 0; m < 4; ++m) {
                    const int seg = mt * 4 + ps * 2 + (m >> 1), kt = m & 1;
#pragma unroll
                    for (int u = 0; u < 2; ++u) {
                        const v4u o = {pk2(acc[m][u][0], acc[m][u][1]), pk2(acc[m][u + 2][0], acc[m][u + 2][1]), pk2(acc[m][u][2], acc[m][u][3]), pk2(acc[m][u + 2][2], acc[m][u + 2][3])};
                        st16s(rc, l16, (unsigned)OFF_G + (unsigned)((((seg * NH + h) * 2 + kt) * 7 + cb) * 2 + u) * 1024u, o);
                    }
                }
            } else {
#pragma unroll
                for (int sg = 0; sg < 2; ++sg) {
                    const int seg = mt * 4 + ps * 2 + sg;
#pragma unroll
                    for (int t = 0; t < 4; ++t) st16s(rc, l16, (unsigned)OFF_U + (unsigned)((seg * NH + h) * 28 + 4 * cb + t) * 1024u, pk8(acc[2 * sg][t], acc[2 * sg + 1][t]));
                }
            }
        }
        signal_add(flags + FL_GU + mt * 32, wave);
    }
}
DI void role_gemm(const Params& p, int wi, unsigned char* lds, int wave) {
    if (wi >= 64) role_ln(p, 191 - wi, wave);
    if (wi >= 168) return;
    const int k = wi / 6, xs = wi % 6;
    const int nt = k < 27 ? xs * 9 + k % 9 : 54 + xs / 3, gq = k < 27 ? k / 9 : xs % 3;
    const int blk = nt * 4 + wave;
    if (blk < 112) gemm_rounds<true>(p, gq, lds, blk / 7, blk % 7, wave);
    else gemm_rounds<false>(p, gq, lds, (blk - 112) / 7, (blk - 112) % 7, wave);
}

DI void role_e(const Params& p, int ei, int wave) {
    const int lane = lane_id(), tid = wave * 64 + lane;
    const int n16 = lane & 15, g = lane >> 4;
    const unsigned l16 = lane * 16;
    unsigned* flags = (unsigned*)(p.ws + OFF_FLAGS);
    const __amdgpu_buffer_rsrc_t rc = __builtin_amdgcn_make_buffer_rsrc(p.ws, 0, (int)WS_END, 0x00020000);
    const f32x4 zero = {0, 0, 0, 0};
    int ready = -1;
#pragma unroll 1
    for (int task = ei * 4 + wave; task < NSEG * 32; task += 96) {
        const int s = task >> 5, h = (task >> 1) & 15, tt = task & 1;
        const int need = s < 8 ? 16 + s : s >> 2;
        if (need != ready) {
            unsigned spins = 0;
            while ((unsigned)__builtin_amdgcn_readfirstlane(__hip_atomic_load(flags + FL_GU + need * 32, __ATOMIC_RELAXED, __HIP_MEMORY_SCOPE_AGENT)) < 56u) {
                __builtin_amdgcn_s_sleep(16);
                if (spin_fail(spins, flags)) break;
            }
            ready = need;
        }
        const unsigned go = (unsigned)OFF_G + (unsigned)((s * NH + h) * 2) * 14336u, ho = (unsigned)OFF_HPI + (unsigned)(2 * s + tt) * 14336u;
        v4u hf[14], g0[14], g1[14];
#pragma unroll
        for (int k = 0; k < 14; ++k) { hf[k] = ld16s(rc, l16, ho + k * 1024); g0[k] = ld16s(rc, l16, go + k * 1024); g1[k] = ld16s(rc, l16, go + 14336 + k * 1024); }
        __builtin_amdgcn_sched_barrier(0);
        f32x4 d0 = zero, d1 = zero;
#pragma unroll
        for (int k = 0; k < 14; ++k) { d0 = mfma(g0[k], hf[k], d0); d1 = mfma(g1[k], hf[k], d1); }
        const int tok = tt * 16 + n16;
        float x[8];
#pragma unroll
        for (int r = 0; r < 4; ++r) { x[r] = (4 * g + r <= tok) ? d0[r] : -INFINITY; x[4 + r] = (16 + 4 * g + r <= tok) ? d1[r] : -INFINITY; }
        float mx = fmaxf(fmaxf(fmaxf(x[0], x[1]), fmaxf(x[2], x[3])), fmaxf(fmaxf(x[4], x[5]), fmaxf(x[6], x[7])));
        mx = xmax(mx);
        float sm = 0.f;
#pragma unroll
        for (int j = 0; j < 8; ++j) { x[j] = __builtin_amdgcn_exp2f(x[j] - mx); sm += x[j]; }
        sm = xsum(sm);
        const float inv = __builtin_amdgcn_rcpf(sm);
        st16s(rc, l16, (unsigned)OFF_SXX + (unsigned)((s * NH + h) * 2 + tt) * 1024u,
              (v4u){pk2(x[0] * inv, x[1] * inv), pk2(x[2] * inv, x[3] * inv), pk2(x[4] * inv, x[5] * inv), pk2(x[6] * inv, x[7] * inv)});
        if (g == 0) __builtin_amdgcn_raw_buffer_store_b32(__float_as_uint(mx + __builtin_amdgcn_logf(sm)), rc, (unsigned)OFF_LSE + (unsigned)(s * NH + h) * 128u + tok * 4, 0, 16);
        asm volatile("s_waitcnt vmcnt(0)" ::: "memory");
        if (lane == 0) __hip_atomic_fetch_add(flags + FL_E + s * 32, 1u, __ATOMIC_RELAXED, __HIP_MEMORY_SCOPE_AGENT);
    }
}

DI void role_tok(const Params& p, int grp, int q, unsigned char* lds, int wave) {
    const int lane = lane_id(), tid = wave * 64 + lane;
    const int n16 = lane & 15, g = lane >> 4;
    const unsigned l16 = lane * 16;
    unsigned* flags = (unsigned*)(p.ws + OFF_FLAGS);
    const __amdgpu_buffer_rsrc_t rc = __builtin_amdgcn_make_buffer_rsrc(p.ws, 0, (int)WS_END, 0x00020000);
    const float* mod = (const float*)(p.ws + OFF_MOD);
    const f32x4 zero = {0, 0, 0, 0};
    const unsigned vsx = (g >> 1) * 2048 + n16 * 16 + (g & 1) * 8, vls = (g >> 1) * 256 + n16 * 4;
    v4u w1f[2][14], w2f[2][14];
    {
        const int qq = n16 >> 2, pp = n16 & 3;
        v4u stg[28];
#pragma unroll
        for (int k = 0; k < 28; ++k) { const int i = tid + k * 256; stg[k] = *(const v4u*)(p.w1 + (size_t)(i >> 4) * FF + 128 * q + 8 * (i & 15)); }
#pragma unroll
        for (int k = 0; k < 28; ++k) { const int i = tid + k * 256; *(v4u*)(lds + (i >> 4) * 272 + (i & 15) * 16) = stg[k]; }
#pragma unroll
        for (int k = 0; k < 28; ++k) { const int i = tid + k * 256; stg[k] = *(const v4u*)(p.w2 + (size_t)(i >> 2) * DM + 32 * q + 8 * (i & 3)); }
        __syncthreads();
#pragma unroll
        for (int j = 0; j < 2; ++j)
#pragma unroll
            for (int k = 0; k < 14; ++k) {
                const LAS unsigned char* a = (const LAS unsigned char*)lds + (32 * k + 4 * g + qq) * 272 + (32 * wave + 16 * j + 4 * pp) * 2;
                const v2u lo = trread(a), hi = trread(a + 16 * 272);
                w1f[j][k] = (v4u){lo.x, lo.y, hi.x, hi.y};
            }
        __syncthreads();
#pragma unroll
        for (int k = 0; k < 28; ++k) {
            const int i = tid + k * 256; const v4u t = stg[k];
            *(v2u*)(lds + (i >> 2) * 72 + (i & 3) * 16) = (v2u){t.x, t.y}; *(v2u*)(lds + (i >> 2) * 72 + (i & 3) * 16 + 8) = (v2u){t.z, t.w};
        }
        __syncthreads();
#pragma unroll
        for (int j = 0; j < 2; ++j)
#pragma unroll
            for (int k = 0; k < 14; ++k) {
                const LAS unsigned char* a = (const LAS unsigned char*)lds + (32 * (14 * wave + k) + 4 * g + qq) * 72 + (16 * j + 4 * pp) * 2;
                const v2u lo = trread(a), hi = trread(a + 16 * 72);
                w2f[j][k] = (v4u){lo.x, lo.y, hi.x, hi.y};
            }
        __syncthreads();
    }
    for (int i = tid; i < 2 * DM; i += 256) ((float*)(lds + T_MOD))[i] = i < DM ? mod[3 * DM + i] : 1.f + mod[4 * DM + i - DM];
    f32x4 b1v[2], gav[2], gbv[2], b2v[2];
#pragma unroll
    for (int j = 0; j < 2; ++j) {
        const v2u bb = *(const v2u*)(p.b1 + 128 * q + 32 * wave + 16 * j + 4 * g);
        b1v[j] = (f32x4){bflo(bb.x), bfhi(bb.x), bflo(bb.y), bfhi(bb.y)};
        const int c = 32 * q + 16 * j + 4 * g;
        gav[j] = *(const f32x4*)(mod + 2 * DM + c); gbv[j] = *(const f32x4*)(mod + 5 * DM + c);
        const v2u b2 = *(const v2u*)(p.b2 + c);
        b2v[j] = (f32x4){bflo(b2.x), bfhi(b2.x), bflo(b2.y), bfhi(b2.y)};
    }
#pragma unroll 1
    for (int s = grp; s < NSEG; s += NGRP) {
        wait_ge(flags + FL_GU + (s < 8 ? 16 + s : s >> 2) * 32, 56, flags, wave);
        v4u uf[4][2];
#pragma unroll
        for (int hh = 0; hh < 4; ++hh)
#pragma unroll
            for (int ct = 0; ct < 2; ++ct) uf[hh][ct] = ld16s(rc, l16, (unsigned)OFF_U + (unsigned)((s * NH + 4 * wave + hh) * 28 + 2 * q + ct) * 1024u);
        v2u xv[2] = {{0, 0}, {0, 0}};
        if (wave < 2) {
#pragma unroll
            for (int j = 0; j < 2; ++j) xv[j] = *(const v2u*)(p.x + (size_t)(s * 32 + wave * 16 + n16) * DM + 32 * q + 16 * j + 4 * g);
        }
        wait_ge(flags + FL_E + s * 32, 32, flags, wave);
        f32x4 T[4][2][2];
        float lse[2][2];
        {
            v4u e[4][2];
#pragma unroll
            for (int hh = 0; hh < 4; ++hh)
#pragma unroll
                for (int tt = 0; tt < 2; ++tt) e[hh][tt] = ld16s(rc, l16, (unsigned)OFF_SXX + (unsigned)((s * NH + 4 * wave + hh) * 2 + tt) * 1024u);
            __builtin_amdgcn_sched_barrier(0);
#pragma unroll
            for (int hh = 0; hh < 4; ++hh)
#pragma unroll
                for (int tt = 0; tt < 2; ++tt)
#pragma unroll
                    for (int ct = 0; ct < 2; ++ct) T[hh][tt][ct] = mfma(uf[hh][ct], e[hh][tt], zero);
        }
#pragma unroll
        for (int ab = 0; ab < 2; ++ab)
#pragma unroll
            for (int tt = 0; tt < 2; ++tt)
                lse[ab][tt] = __uint_as_float(__builtin_amdgcn_raw_buffer_load_b32(rc, vls, (unsigned)OFF_LSE + (unsigned)(s * NH + 4 * wave + ab) * 128u + tt * 64, 16));
        const bool fast = s >= FAST0, lastseg = s == NSEG - 1;
        wait_rec(flags, lastseg ? (unsigned)NSEG - 1u : s + 2u, wave);
        v2u sm0[2][2], sm1[2][2]; v4u um[2][2];
        for (unsigned spins = 0;;) {
#pragma unroll
            for (int ab = 0; ab < 2; ++ab)
#pragma unroll
                for (int tt = 0; tt < 2; ++tt) {
                    const unsigned o = (unsigned)OFF_SXM + (unsigned)((s * NH + 4 * wave + ab) * 2) * 512u + tt * 256;
                    sm0[ab][tt] = ld8s(rc, vsx, o); sm1[ab][tt] = ld8s(rc, vsx, o + 512);
                }
#pragma unroll
            for (int ct = 0; ct < 2; ++ct)
#pragma unroll
                for (int ab = 0; ab < 2; ++ab) um[ct][ab] = ld16s(rc, l16, (unsigned)OFF_UMF + (unsigned)((s * 28 + 2 * q + ct) * 4 + wave) * 2048u + ab * 1024);
            if (!lastseg) break;
            unsigned m = 0;
#pragma unroll
            for (int ab = 0; ab < 2; ++ab)
#pragma unroll
                for (int tt = 0; tt < 2; ++tt) m = max(max(m, mxu(um[ab][tt])), max(max(sm0[ab][tt].x, sm0[ab][tt].y), max(sm1[ab][tt].x, sm1[ab][tt].y)));
            if (__all(m != SENT)) break;
            __builtin_amdgcn_s_sleep(3);
            if (spin_fail(spins, flags)) break;
        }
        f32x4 acc[2][2] = {{zero, zero}, {zero, zero}};
#pragma unroll
        for (int ab = 0; ab < 2; ++ab)
#pragma unroll
            for (int tt = 0; tt < 2; ++tt) {
                const float a0 = bflo(sm0[ab][tt].x) + bflo(sm1[ab][tt].x), a1 = bfhi(sm0[ab][tt].x) + bfhi(sm1[ab][tt].x);
                const float a2 = bflo(sm0[ab][tt].y) + bflo(sm1[ab][tt].y), a3 = bfhi(sm0[ab][tt].y) + bfhi(sm1[ab][tt].y);
                const float l = lse[ab][tt];
                const float mx = sw16max(fmaxf(fmaxf(fmaxf(a0, a1), fmaxf(a2, a3)), l));
                const float e0 = __builtin_amdgcn_exp2f(a0 - mx), e1 = __builtin_amdgcn_exp2f(a1 - mx), e2 = __builtin_amdgcn_exp2f(a2 - mx), e3 = __builtin_amdgcn_exp2f(a3 - mx);
                const float wx = __builtin_amdgcn_exp2f(l - mx);
                const float inv = __builtin_amdgcn_rcpf(sw16add((e0 + e1) + (e2 + e3)) + wx);
                const unsigned p0 = pk2(e0 * inv, e1 * inv), p1 = pk2(e2 * inv, e3 * inv), sc = __float_as_uint(wx * inv);
                const auto q0 = __builtin_amdgcn_permlane32_swap(p0, p0, false, false);
                const auto q1 = __builtin_amdgcn_permlane32_swap(p1, p1, false, false);
                const auto qs = __builtin_amdgcn_permlane32_swap(sc, sc, false, false);
                const v4u pm = {q0[0], q1[0], q0[1], q1[1]};
                const float s0 = __uint_as_float(qs[0]), s1 = __uint_as_float(qs[1]);
#pragma unroll
                for (int ct = 0; ct < 2; ++ct) {
                    acc[tt][ct] += T[ab][tt][ct] * s0 + T[2 + ab][tt][ct] * s1;
                    acc[tt][ct] = mfma(um[ct][ab], pm, acc[tt][ct]);
                }
            }
#pragma unroll
        for (int tt = 0; tt < 2; ++tt)
#pragma unroll
            for (int ct = 0; ct < 2; ++ct) *(f32x4*)(lds + T_RED + ((wave * 2 + tt) * 2 + ct) * 1024 + lane * 16) = acc[tt][ct];
        __syncthreads();
        f32x4 x1[2] = {zero, zero};
        if (wave < 2) {
#pragma unroll
            for (int ct = 0; ct < 2; ++ct) {
                f32x4 a = zero;
#pragma unroll
                for (int w = 0; w < 4; ++w) a += *(const f32x4*)(lds + T_RED + ((w * 2 + wave) * 2 + ct) * 1024 + lane * 16);
                const f32x4 xx = {bflo(xv[ct].x), bfhi(xv[ct].x), bflo(xv[ct].y), bfhi(xv[ct].y)};
                x1[ct] = xx + gav[ct] * a;
            }
            const v4u xb = pk8(x1[0], x1[1]);
            x1[0] = (f32x4){bflo(xb.x), bfhi(xb.x), bflo(xb.y), bfhi(xb.y)}; x1[1] = (f32x4){bflo(xb.z), bfhi(xb.z), bflo(xb.w), bfhi(xb.w)};
            st16s(rc, l16, (unsigned)OFF_X1F + (unsigned)((s * 2 + wave) * 14 + q) * 1024u, nosent4(xb));
        }
        if (!fast) { signal_add(flags + FL_X1 + s * 32, wave); wait_ge(flags + FL_X1 + s * 32, 14, flags, wave); }
        else { __syncthreads(); __builtin_amdgcn_s_sleep(6); }
        {
            const int tt = wave & 1, kh = wave >> 1;
            v4u xf[7];
            for (unsigned spins = 0;;) {
#pragma unroll
                for (int k = 0; k < 7; ++k) xf[k] = ld16s(rc, l16, (unsigned)OFF_X1F + (unsigned)((s * 2 + tt) * 14 + kh * 7 + k) * 1024u);
                if (!fast) break;
                unsigned m = 0;
#pragma unroll
                for (int k = 0; k < 7; ++k) m = max(m, mxu(xf[k]));
                if (__all(m != SENT)) break;
                __builtin_amdgcn_s_sleep(2);
                if (spin_fail(spins, flags)) break;
            }
            float f[56];
#pragma unroll
            for (int k = 0; k < 7; ++k)
#pragma unroll
                for (int j = 0; j < 4; ++j) { f[k * 8 + 2 * j] = bflo(xf[k][j]); f[k * 8 + 2 * j + 1] = bfhi(xf[k][j]); }
            float sum = 0.f;
#pragma unroll
            for (int i = 0; i < 56; ++i) sum += f[i];
            float* st = (float*)(lds + T_ST);
            st[wave * 64 + lane] = xsum(sum);
            __syncthreads();
            const float mu = (st[wave * 64 + lane] + st[(wave ^ 2) * 64 + lane]) * (1.f / DM);
            float var = 0.f;
#pragma unroll
            for (int i = 0; i < 56; ++i) { f[i] -= mu; var += f[i] * f[i]; }
            st[256 + wave * 64 + lane] = xsum(var);
            __syncthreads();
            const float rs = 1.f / sqrtf((st[256 + wave * 64 + lane] + st[256 + (wave ^ 2) * 64 + lane]) * (1.f / DM) + 1e-5f);
            const float* tm = (const float*)(lds + T_MOD) + kh * 224 + 4 * g;
#pragma unroll
            for (int k = 0; k < 7; ++k) {
                const f32x4 s0 = *(const f32x4*)(tm + 32 * k), s1 = *(const f32x4*)(tm + 32 * k + 16);
                const f32x4 c0 = *(const f32x4*)(tm + DM + 32 * k) * rs, c1 = *(const f32x4*)(tm + DM + 32 * k + 16) * rs;
                v4u o;
                o.x = pk2(f[k * 8 + 0] * c0[0] + s0[0], f[k * 8 + 1] * c0[1] + s0[1]);
                o.y = pk2(f[k * 8 + 2] * c0[2] + s0[2], f[k * 8 + 3] * c0[3] + s0[3]);
                o.z = pk2(f[k * 8 + 4] * c1[0] + s1[0], f[k * 8 + 5] * c1[1] + s1[1]);
                o.w = pk2(f[k * 8 + 6] * c1[2] + s1[2], f[k * 8 + 7] * c1[3] + s1[3]);
                *(v4u*)(lds + T_H2 + (tt * 14 + kh * 7 + k) * 1024 + lane * 16) = o;
            }
        }
        __syncthreads();
        {
            f32x4 f[2][2] = {{b1v[0], b1v[1]}, {b1v[0], b1v[1]}};
            ACC_READY4(f[0][0], f[0][1], f[1][0], f[1][1]);
            v4u hf[2][14];
#pragma unroll
            for (int k = 0; k < 14; ++k)
#pragma unroll
                for (int tt = 0; tt < 2; ++tt) hf[tt][k] = *(const v4u*)(lds + T_H2 + (tt * 14 + k) * 1024 + lane * 16);
            __builtin_amdgcn_sched_barrier(0);
#pragma unroll
            for (int k = 0; k < 14; ++k)
#pragma unroll
                for (int tt = 0; tt < 2; ++tt)
#pragma unroll
                    for (int j = 0; j < 2; ++j) MF_AV(f[tt][j], w1f[j][k], hf[tt][k]);
            ACC_FENCE4(f[0][0], f[0][1], f[1][0], f[1][1]);
#pragma unroll
            for (int tt = 0; tt < 2; ++tt) {
#pragma unroll
                for (int j = 0; j < 2; ++j)
#pragma unroll
                    for (int r = 0; r < 4; ++r) {
                        const float v = f[tt][j][r];
                        const float u = v * (1.f + 0.044715f * v * v) * (-2.f * 0.7978845608028654f * 1.4426950408889634f);
                        f[tt][j][r] = v * __builtin_amdgcn_rcpf(1.f + __builtin_amdgcn_exp2f(u));
                    }
                st16s(rc, l16, (unsigned)OFF_FFR + (unsigned)((s * 2 + tt) * 56 + 4 * q + wave) * 1024u, nosent4(pk8(f[tt][0], f[tt][1])));
            }
        }
        if (!fast) { signal_add(flags + FL_F + s * 32, wave); wait_ge(flags + FL_F + s * 32, 14, flags, wave); }
        else { __syncthreads(); __builtin_amdgcn_s_sleep(6); }
        {
            f32x4 y[2][2] = {{zero, zero}, {zero, zero}};
            ACC_READY4(y[0][0], y[0][1], y[1][0], y[1][1]);
            v4u ff[2][14];
            for (unsigned spins = 0;;) {
#pragma unroll
                for (int tt = 0; tt < 2; ++tt)
#pragma unroll
                    for (int k = 0; k < 14; ++k) ff[tt][k] = ld16s(rc, l16, (unsigned)OFF_FFR + (unsigned)((s * 2 + tt) * 56 + 14 * wave + k) * 1024u);
                if (!fast) break;
                unsigned m = 0;
#pragma unroll
                for (int tt = 0; tt < 2; ++tt)
#pragma unroll
                    for (int k = 0; k < 14; ++k) m = max(m, mxu(ff[tt][k]));
                if (__all(m != SENT)) break;
                __builtin_amdgcn_s_sleep(2);
                if (spin_fail(spins, flags)) break;
            }
            __builtin_amdgcn_sched_barrier(0);
#pragma unroll
            for (int tt = 0; tt < 2; ++tt)
#pragma unroll
                for (int k = 0; k < 14; ++k)
#pragma unroll
                    for (int ct = 0; ct < 2; ++ct) MF_AV(y[tt][ct], w2f[ct][k], ff[tt][k]);
            ACC_FENCE4(y[0][0], y[0][1], y[1][0], y[1][1]);
#pragma unroll
            for (int tt = 0; tt < 2; ++tt)
#pragma unroll
                for (int ct = 0; ct < 2; ++ct) *(f32x4*)(lds + T_RED + ((wave * 2 + tt) * 2 + ct) * 1024 + lane * 16) = y[tt][ct];
        }
        __syncthreads();
        if (wave < 2) {
#pragma unroll
            for (int ct = 0; ct < 2; ++ct) {
                f32x4 a = b2v[ct];
#pragma unroll
                for (int w = 0; w < 4; ++w) a += *(const f32x4*)(lds + T_RED + ((w * 2 + wave) * 2 + ct) * 1024 + lane * 16);
                const f32x4 o = x1[ct] + gbv[ct] * a;
                *(v2u*)(p.out + (size_t)(s * 32 + wave * 16 + n16) * DM + 32 * q + 16 * ct + 4 * g) = (v2u){pk2(o[0], o[1]), pk2(o[2], o[3])};
            }
        }
        __syncthreads();
    }
}

constexpr int K1_LDS = 141312;
static_assert(L_REC_END <= K1_LDS && G_END <= K1_LDS && 1792 * 72 <= K1_LDS && 448 * 272 <= K1_LDS, "lds");
__global__ void __launch_bounds__(256, 1) k1_main(Params p) {
    p.ws = align_ws(p.ws);
    extern __shared__ __attribute__((aligned(16))) unsigned char lds[];
    const int b = blockIdx.x, wave = __builtin_amdgcn_readfirstlane(threadIdx.x >> 6);
    if ((b & 7) == 0) { role_rec<false>(p, b >> 3, lds, wave); return; }
    if ((b & 7) == REPX) { role_rec<true>(p, b >> 3, lds, wave); return; }
    const int xs = (b & 7) - 1 - ((b & 7) > REPX ? 1 : 0), k = b >> 3;
    role_gemm(p, k * 6 + xs, lds, wave);
    if (k >= 28) { role_e(p, (k - 28) * 6 + xs, wave); return; }
    __syncthreads();
    role_tok(p, xs * 2 + k / 14, k % 14, lds, wave);
}

extern "C" void kernel_launch(void* const* d_in, const int* in_sizes, int n_in, void* d_out, int out_size, void* d_ws, size_t ws_size, hipStream_t stream) {
    Params p;
    p.x = (const u16*)d_in[0]; p.mem0 = (const u16*)d_in[1]; p.Wq = (const u16*)d_in[2]; p.Wk = (const u16*)d_in[3];
    p.Wv = (const u16*)d_in[4]; p.Wo = (const u16*)d_in[5]; p.w1 = (const u16*)d_in[8]; p.b1 = (const u16*)d_in[9];
    p.w2 = (const u16*)d_in[10]; p.b2 = (const u16*)d_in[11]; p.c = (const u16*)d_in[12]; p.wmod = (const u16*)d_in[13];
    p.out = (u16*)d_out;
    p.ws = (unsigned char*)d_ws;
    k0_prep<<<K0_WGS, 448, 0, stream>>>(p);
    (void)hipFuncSetAttribute((const void*)k1_main, hipFuncAttributeMaxDynamicSharedMemorySize, K1_LDS);
    k1_main<<<256, 256, K1_LDS, stream>>>(p);
}
```
